```python
import torch

def _softmax(x, axis=-1):
    return torch.softmax(x, dim=axis)
def _gelu(x):
    return torch.nn.functional.gelu(x, approximate="tanh")
def _silu(x):
    return torch.nn.functional.silu(x)
def _sigmoid(x):
    return torch.sigmoid(x)
def _layernorm(x, axis=-1, eps=1e-5):
    m = x.mean(dim=axis, keepdim=True)
    v = x.var(dim=axis, keepdim=True, unbiased=False)
    return (x - m) / torch.sqrt(v + eps)
def _scatter_add(tgt, idx, src, axis=0):
    return tgt.clone().index_add_(axis, idx, src)
def _segment_sum(src, seg, n):
    out = torch.zeros((n,) + src.shape[1:], dtype=src.dtype)
    return out.index_add_(0, seg, src)
def _pad(x, pw):
    flat = tuple(v for pair in reversed(pw) for v in pair)
    return torch.nn.functional.pad(x, flat)

kernel_name = 'standalone__mixer_token_mlp__prompt'

def setup_inputs():

    _w = (lambda a: (lambda t: t.to(torch.bfloat16) if t.is_floating_point() else t)(torch.as_tensor(a)))
    B, S, D, F = 1, 2048, 512, 1376
    return {
        "x": _w((lambda *s: torch.randn(*s, dtype=torch.bfloat16) * 0.05)(B, S, D)),
        "w1": _w((lambda *s: torch.randn(*s, dtype=torch.bfloat16) * 0.2)(S, F) * (S ** -0.5)),
        "b1": _w((lambda *s: torch.randn(*s, dtype=torch.bfloat16) * 1.6)(F) * 0.02),
        "w2": _w((lambda *s: torch.randn(*s, dtype=torch.bfloat16) * 0.05)(F, S) * (F ** -0.5)),
        "b2": _w((lambda *s: torch.randn(*s, dtype=torch.bfloat16) * 0.1)(S) * 0.02),
    }

def reference(x, w1, b1, w2, b2):
    B, S, D = x.shape
    h = _layernorm(x, axis=-1)
    u = torch.einsum("bsd,sf->bdf", h, w1) + b1
    u = _gelu(u)
    y = torch.einsum("bdf,fs->bds", u, w2) + b2
    return (lambda x, *d: x.permute(*d))(y, 0, 2, 1)
```

```cpp
#include <hip/hip_runtime.h>
#include <hip/hip_bf16.h>
#include <stdint.h>

typedef short bf16x8 __attribute__((ext_vector_type(8)));
typedef short s16x4 __attribute__((ext_vector_type(4)));
typedef float f32x4 __attribute__((ext_vector_type(4)));
typedef unsigned u32x4 __attribute__((ext_vector_type(4)));
typedef unsigned u32x2 __attribute__((ext_vector_type(2)));

constexpr int S = 2048, D = 512, F = 1376;

__device__ __forceinline__ float bf2f(unsigned short b) { return __builtin_bit_cast(float, (unsigned)b << 16); }
__device__ __forceinline__ unsigned short f2bf(float f) { __bf16 b = (__bf16)f; return __builtin_bit_cast(unsigned short, b); }

__global__ __launch_bounds__(256) void k_ln(const unsigned short* __restrict__ x, unsigned short* __restrict__ hf) {
  int wave = threadIdx.x >> 6, lane = threadIdx.x & 63;
  int s = blockIdx.x * 4 + wave;
  u32x4 v = *(const u32x4*)(x + (size_t)s * D + lane * 8);
  float f[8];
  for (int i = 0; i < 4; ++i) { f[2*i] = __builtin_bit_cast(float, v[i] << 16); f[2*i+1] = __builtin_bit_cast(float, v[i] & 0xffff0000u); }
  float sum = 0; for (int i = 0; i < 8; ++i) sum += f[i];
  for (int o = 32; o; o >>= 1) sum += __shfl_xor(sum, o);
  float m = sum * (1.0f / D);
  float q = 0; for (int i = 0; i < 8; ++i) { float t = f[i] - m; q += t * t; }
  for (int o = 32; o; o >>= 1) q += __shfl_xor(q, o);
  float r = rsqrtf(q * (1.0f / D) + 1e-5f);
  int ks = s >> 5, kq = (s >> 3) & 3, j = s & 7;
  for (int i = 0; i < 8; ++i) {
    int d = lane * 8 + i;
    int dt = d >> 4, dr = d & 15;
    hf[(((size_t)dt * 64 + ks) * 64 + kq * 16 + dr) * 8 + j] = f2bf((f[i] - m) * r);
  }
}

__device__ __forceinline__ float gelu_t(float u) {
  float z = 0.7978845608028654f * (u + 0.044715f * u * u * u);
  float e = __expf(2.0f * z);
  float t = 1.0f - 2.0f / (e + 1.0f);
  return 0.5f * u * (1.0f + t);
}

template <int KSTEPS, int NB, bool FIRST>
__global__ __launch_bounds__(256) void k_gemm(const unsigned short* __restrict__ Af, const unsigned short* __restrict__ B,
                                              const unsigned short* __restrict__ bias, unsigned short* __restrict__ out) {
  __shared__ __attribute__((aligned(16))) unsigned short sb[4][32][64 + 8];
  __shared__ __attribute__((aligned(16))) float red[4][64][65];
  int wave = threadIdx.x >> 6, lane = threadIdx.x & 63;
  int mt = blockIdx.x & 7, nt = blockIdx.x >> 3;
  int n0 = nt * 64;
  f32x4 acc[4][4] = {};
  for (int ks = wave; ks < KSTEPS; ks += 4) {
    for (int i = 0; i < 4; ++i) {
      int r = i * 8 + (lane >> 3), c = (lane & 7) * 8;
      u32x4 v = {0,0,0,0};
      if (n0 + c < NB) v = *(const u32x4*)(B + (size_t)(ks * 32 + r) * NB + n0 + c);
      *(u32x4*)&sb[wave][r][c] = v;
    }
    bf16x8 a[4], b[4];
    for (int m = 0; m < 4; ++m) a[m] = *(const bf16x8*)(Af + (((size_t)(mt * 4 + m) * KSTEPS + ks) * 64 + lane) * 8);
    __builtin_amdgcn_s_waitcnt(0);
    __builtin_amdgcn_wave_barrier();
    for (int n = 0; n < 4; ++n)
      for (int j = 0; j < 8; ++j) b[n][j] = (short)sb[wave][(lane >> 4) * 8 + j][n * 16 + (lane & 15)];
    for (int m = 0; m < 4; ++m) for (int n = 0; n < 4; ++n)
      acc[m][n] = __builtin_amdgcn_mfma_f32_16x16x32_bf16(a[m], b[n], acc[m][n], 0, 0, 0);
    __builtin_amdgcn_wave_barrier();
  }
  for (int m = 0; m < 4; ++m) for (int n = 0; n < 4; ++n) for (int j = 0; j < 4; ++j)
    red[wave][m * 16 + (lane >> 4) * 4 + j][n * 16 + (lane & 15)] = acc[m][n][j];
  __syncthreads();
  for (int i = threadIdx.x; i < 64 * 64; i += 256) {
    if (FIRST) {
      int n = i & 63, m = i >> 6;
      int f = n0 + n; if (f >= NB) continue;
      float u = red[0][m][n] + red[1][m][n] + red[2][m][n] + red[3][m][n] + bf2f(bias[f]);
      float g = gelu_t(u);
      int d = mt * 64 + m;
      out[((((size_t)(d >> 4)) * (F / 32) + (f >> 5)) * 64 + ((f >> 3) & 3) * 16 + (d & 15)) * 8 + (f & 7)] = f2bf(g);
    } else {
      int m = i & 63, n = i >> 6;
      int s = n0 + n, d = mt * 64 + m;
      float u = red[0][m][n] + red[1][m][n] + red[2][m][n] + red[3][m][n] + bf2f(bias[s]);
      out[(size_t)s * D + d] = f2bf(u);
    }
  }
}

extern "C" void kernel_launch(void* const* d_in, const int* in_sizes, int n_in, void* d_out, int out_size, void* d_ws,
                              size_t ws_size, hipStream_t stream) {
  const unsigned short* x = (const unsigned short*)d_in[0];
  const unsigned short* w1 = (const unsigned short*)d_in[1];
  const unsigned short* b1 = (const unsigned short*)d_in[2];
  const unsigned short* w2 = (const unsigned short*)d_in[3];
  const unsigned short* b2 = (const unsigned short*)d_in[4];
  unsigned short* hf = (unsigned short*)d_ws;
  unsigned short* gf = hf + (size_t)S * D;
  k_ln<<<S / 4, 256, 0, stream>>>(x, hf);
  k_gemm<S / 32, F, true><<<8 * 22, 256, 0, stream>>>(hf, w1, b1, gf);
  k_gemm<F / 32, S, false><<<8 * 32, 256, 0, stream>>>(gf, w2, b2, (unsigned short*)d_out);
}
```

```cpp
#include <hip/hip_runtime.h>
#include <hip/hip_bf16.h>
#include <stdint.h>

typedef short bf16x8 __attribute__((ext_vector_type(8)));
typedef short s16x4 __attribute__((ext_vector_type(4)));
typedef float f32x4 __attribute__((ext_vector_type(4)));
typedef unsigned u32x4 __attribute__((ext_vector_type(4)));
typedef unsigned u32x2 __attribute__((ext_vector_type(2)));

constexpr int S = 2048, D = 512, F = 1376;

__device__ __forceinline__ float bf2f(unsigned short b) { return __builtin_bit_cast(float, (unsigned)b << 16); }
__device__ __forceinline__ unsigned short f2bf(float f) { __bf16 b = (__bf16)f; return __builtin_bit_cast(unsigned short, b); }

__global__ __launch_bounds__(256) void k_ln(const unsigned short* __restrict__ x, unsigned short* __restrict__ hf) {
  int wave = threadIdx.x >> 6, lane = threadIdx.x & 63;
  int s = blockIdx.x * 4 + wave;
  u32x4 v = *(const u32x4*)(x + (size_t)s * D + lane * 8);
  float f[8];
  for (int i = 0; i < 4; ++i) { f[2*i] = __builtin_bit_cast(float, v[i] << 16); f[2*i+1] = __builtin_bit_cast(float, v[i] & 0xffff0000u); }
  float sum = 0; for (int i = 0; i < 8; ++i) sum += f[i];
  for (int o = 32; o; o >>= 1) sum += __shfl_xor(sum, o);
  float m = sum * (1.0f / D);
  float q = 0; for (int i = 0; i < 8; ++i) { float t = f[i] - m; q += t * t; }
  for (int o = 32; o; o >>= 1) q += __shfl_xor(q, o);
  float r = rsqrtf(q * (1.0f / D) + 1e-5f);
  int ks = s >> 5, kq = (s >> 3) & 3, j = s & 7;
  for (int i = 0; i < 8; ++i) {
    int d = lane * 8 + i;
    int dt = d >> 4, dr = d & 15;
    hf[(((size_t)dt * 64 + ks) * 64 + kq * 16 + dr) * 8 + j] = f2bf((f[i] - m) * r);
  }
}

__device__ __forceinline__ float gelu_t(float u) {
  float z = 0.7978845608028654f * (u + 0.044715f * u * u * u);
  float e = __expf(2.0f * z);
  float t = 1.0f - 2.0f / (e + 1.0f);
  return 0.5f * u * (1.0f + t);
}

template <int KSTEPS, int NB, bool FIRST>
__global__ __launch_bounds__(256) void k_gemm(const unsigned short* __restrict__ Af, const unsigned short* __restrict__ B,
                                              const unsigned short* __restrict__ bias, unsigned short* __restrict__ out) {
  __shared__ __attribute__((aligned(16))) unsigned short sb[4][32][64 + 8];
  __shared__ __attribute__((aligned(16))) float red[4][64][65];
  int wave = threadIdx.x >> 6, lane = threadIdx.x & 63;
  int mt = blockIdx.x & 7, nt = blockIdx.x >> 3;
  int n0 = nt * 64;
  f32x4 acc[4][4] = {};
  for (int ks = wave; ks < KSTEPS; ks += 4) {
    for (int i = 0; i < 4; ++i) {
      int r = i * 8 + (lane >> 3), c = (lane & 7) * 8;
      u32x4 v = {0,0,0,0};
      if (n0 + c < NB) v = *(const u32x4*)(B + (size_t)(ks * 32 + r) * NB + n0 + c);
      *(u32x4*)&sb[wave][r][c] = v;
    }
    bf16x8 a[4], b[4];
    for (int m = 0; m < 4; ++m) a[m] = *(const bf16x8*)(Af + (((size_t)(mt * 4 + m) * KSTEPS + ks) * 64 + lane) * 8);
    __builtin_amdgcn_s_waitcnt(0);
    __builtin_amdgcn_wave_barrier();
    for (int n = 0; n < 4; ++n)
      for (int j = 0; j < 8; ++j) b[n][j] = (short)sb[wave][(lane >> 4) * 8 + j][n * 16 + (lane & 15)];
    for (int m = 0; m < 4; ++m) for (int n = 0; n < 4; ++n)
      acc[m][n] = __builtin_amdgcn_mfma_f32_16x16x32_bf16(a[m], b[n], acc[m][n], 0, 0, 0);
    __builtin_amdgcn_wave_barrier();
  }
  for (int m = 0; m < 4; ++m) for (int n = 0; n < 4; ++n) for (int j = 0; j < 4; ++j)
    red[wave][m * 16 + (lane >> 4) * 4 + j][n * 16 + (lane & 15)] = acc[m][n][j];
  __syncthreads();
  for (int i = threadIdx.x; i < 64 * 64; i += 256) {
    if (FIRST) {
      int n = i & 63, m = i >> 6;
      int f = n0 + n; if (f >= NB) continue;
      float u = red[0][m][n] + red[1][m][n] + red[2][m][n] + red[3][m][n] + bf2f(bias[f]);
      float g = gelu_t(u);
      int d = mt * 64 + m;
      out[((((size_t)(d >> 4)) * (F / 32) + (f >> 5)) * 64 + ((f >> 3) & 3) * 16 + (d & 15)) * 8 + (f & 7)] = f2bf(g);
    } else {
      int m = i & 63, n = i >> 6;
      int s = n0 + n, d = mt * 64 + m;
      float u = red[0][m][n] + red[1][m][n] + red[2][m][n] + red[3][m][n] + bf2f(bias[s]);
      out[(size_t)s * D + d] = f2bf(u);
    }
  }
}


template <int NT, int N>
__global__ __launch_bounds__(512) void k_probe(const char* __restrict__ p1, const char* __restrict__ p2, unsigned* sink) {
  unsigned long long t0 = __builtin_amdgcn_s_memrealtime();
  const char* base = (blockIdx.x < 128 ? p1 : p2) + (size_t)(blockIdx.x & 127) * (8 * N * 1024) + threadIdx.x * 16;
  u32x4 r[N];
#pragma unroll
  for (int j = 0; j < N; ++j) r[j] = u32x4{0u,0u,0u,0u};
#pragma unroll
  for (int j = 0; j < N; ++j) {
    const char* a = base + j * 8192;
    if (NT) asm volatile("global_load_dwordx4 %0, %1, off nt" : "+v"(r[j]) : "v"(a) : "memory");
    else asm volatile("global_load_dwordx4 %0, %1, off" : "+v"(r[j]) : "v"(a) : "memory");
  }
  asm volatile("s_waitcnt vmcnt(0)" ::: "memory");
#pragma unroll
  for (int j = 0; j < N; ++j) asm volatile("" :: "v"(r[j]));
  __syncthreads();
  unsigned long long t1 = __builtin_amdgcn_s_memrealtime();
  unsigned long long until = t1 + (t1 - t0) * 20;
  while (__builtin_amdgcn_s_memrealtime() < until) __builtin_amdgcn_s_sleep(2);
}
extern "C" void kernel_launch(void* const* d_in, const int* in_sizes, int n_in, void* d_out, int out_size, void* d_ws,
                              size_t ws_size, hipStream_t stream) {
  const unsigned short* x = (const unsigned short*)d_in[0];
  const unsigned short* w1 = (const unsigned short*)d_in[1];
  const unsigned short* b1 = (const unsigned short*)d_in[2];
  const unsigned short* w2 = (const unsigned short*)d_in[3];
  const unsigned short* b2 = (const unsigned short*)d_in[4];
  unsigned short* hf = (unsigned short*)d_ws;
  unsigned short* gf = hf + (size_t)S * D;
  k_probe<1, 1><<<256, 512, 0, stream>>>((const char*)d_in[1], (const char*)d_in[3], (unsigned*)d_ws);
  k_ln<<<S / 4, 256, 0, stream>>>(x, hf);
  k_gemm<S / 32, F, true><<<8 * 22, 256, 0, stream>>>(hf, w1, b1, gf);
  k_gemm<F / 32, S, false><<<8 * 32, 256, 0, stream>>>(gf, w2, b2, (unsigned short*)d_out);
}
```

```cpp
#include <hip/hip_runtime.h>
#include <hip/hip_bf16.h>
#include <stdint.h>

typedef short bf16x8 __attribute__((ext_vector_type(8)));
typedef short s16x4 __attribute__((ext_vector_type(4)));
typedef float f32x4 __attribute__((ext_vector_type(4)));
typedef unsigned u32x4 __attribute__((ext_vector_type(4)));
typedef unsigned u32x2 __attribute__((ext_vector_type(2)));

constexpr int S = 2048, D = 512, F = 1376;

__device__ __forceinline__ float bf2f(unsigned short b) { return __builtin_bit_cast(float, (unsigned)b << 16); }
__device__ __forceinline__ unsigned short f2bf(float f) { __bf16 b = (__bf16)f; return __builtin_bit_cast(unsigned short, b); }

__global__ __launch_bounds__(256) void k_ln(const unsigned short* __restrict__ x, unsigned short* __restrict__ hf) {
  int wave = threadIdx.x >> 6, lane = threadIdx.x & 63;
  int s = blockIdx.x * 4 + wave;
  u32x4 v = *(const u32x4*)(x + (size_t)s * D + lane * 8);
  float f[8];
  for (int i = 0; i < 4; ++i) { f[2*i] = __builtin_bit_cast(float, v[i] << 16); f[2*i+1] = __builtin_bit_cast(float, v[i] & 0xffff0000u); }
  float sum = 0; for (int i = 0; i < 8; ++i) sum += f[i];
  for (int o = 32; o; o >>= 1) sum += __shfl_xor(sum, o);
  float m = sum * (1.0f / D);
  float q = 0; for (int i = 0; i < 8; ++i) { float t = f[i] - m; q += t * t; }
  for (int o = 32; o; o >>= 1) q += __shfl_xor(q, o);
  float r = rsqrtf(q * (1.0f / D) + 1e-5f);
  int ks = s >> 5, kq = (s >> 3) & 3, j = s & 7;
  for (int i = 0; i < 8; ++i) {
    int d = lane * 8 + i;
    int dt = d >> 4, dr = d & 15;
    hf[(((size_t)dt * 64 + ks) * 64 + kq * 16 + dr) * 8 + j] = f2bf((f[i] - m) * r);
  }
}

__device__ __forceinline__ float gelu_t(float u) {
  float z = 0.7978845608028654f * (u + 0.044715f * u * u * u);
  float e = __expf(2.0f * z);
  float t = 1.0f - 2.0f / (e + 1.0f);
  return 0.5f * u * (1.0f + t);
}

template <int KSTEPS, int NB, bool FIRST>
__global__ __launch_bounds__(256) void k_gemm(const unsigned short* __restrict__ Af, const unsigned short* __restrict__ B,
                                              const unsigned short* __restrict__ bias, unsigned short* __restrict__ out) {
  __shared__ __attribute__((aligned(16))) unsigned short sb[4][32][64 + 8];
  __shared__ __attribute__((aligned(16))) float red[4][64][65];
  int wave = threadIdx.x >> 6, lane = threadIdx.x & 63;
  int mt = blockIdx.x & 7, nt = blockIdx.x >> 3;
  int n0 = nt * 64;
  f32x4 acc[4][4] = {};
  for (int ks = wave; ks < KSTEPS; ks += 4) {
    for (int i = 0; i < 4; ++i) {
      int r = i * 8 + (lane >> 3), c = (lane & 7) * 8;
      u32x4 v = {0,0,0,0};
      if (n0 + c < NB) v = *(const u32x4*)(B + (size_t)(ks * 32 + r) * NB + n0 + c);
      *(u32x4*)&sb[wave][r][c] = v;
    }
    bf16x8 a[4], b[4];
    for (int m = 0; m < 4; ++m) a[m] = *(const bf16x8*)(Af + (((size_t)(mt * 4 + m) * KSTEPS + ks) * 64 + lane) * 8);
    __builtin_amdgcn_s_waitcnt(0);
    __builtin_amdgcn_wave_barrier();
    for (int n = 0; n < 4; ++n)
      for (int j = 0; j < 8; ++j) b[n][j] = (short)sb[wave][(lane >> 4) * 8 + j][n * 16 + (lane & 15)];
    for (int m = 0; m < 4; ++m) for (int n = 0; n < 4; ++n)
      acc[m][n] = __builtin_amdgcn_mfma_f32_16x16x32_bf16(a[m], b[n], acc[m][n], 0, 0, 0);
    __builtin_amdgcn_wave_barrier();
  }
  for (int m = 0; m < 4; ++m) for (int n = 0; n < 4; ++n) for (int j = 0; j < 4; ++j)
    red[wave][m * 16 + (lane >> 4) * 4 + j][n * 16 + (lane & 15)] = acc[m][n][j];
  __syncthreads();
  for (int i = threadIdx.x; i < 64 * 64; i += 256) {
    if (FIRST) {
      int n = i & 63, m = i >> 6;
      int f = n0 + n; if (f >= NB) continue;
      float u = red[0][m][n] + red[1][m][n] + red[2][m][n] + red[3][m][n] + bf2f(bias[f]);
      float g = gelu_t(u);
      int d = mt * 64 + m;
      out[((((size_t)(d >> 4)) * (F / 32) + (f >> 5)) * 64 + ((f >> 3) & 3) * 16 + (d & 15)) * 8 + (f & 7)] = f2bf(g);
    } else {
      int m = i & 63, n = i >> 6;
      int s = n0 + n, d = mt * 64 + m;
      float u = red[0][m][n] + red[1][m][n] + red[2][m][n] + red[3][m][n] + bf2f(bias[s]);
      out[(size_t)s * D + d] = f2bf(u);
    }
  }
}


template <int N>
__global__ __launch_bounds__(256) void k_probe(unsigned* sink) {
  unsigned long long t0 = __builtin_amdgcn_s_memrealtime();
  unsigned v = threadIdx.x, a = 1, b = 2;
  asm volatile("s_waitcnt lgkmcnt(0)");
  asm volatile(".rept %c3\n\tv_add3_u32 %0, %0, %1, %2\n\t.endr" : "+v"(v) : "v"(a), "v"(b), "i"(N));
  unsigned long long t1 = __builtin_amdgcn_s_memrealtime();
  unsigned long long until = t1 + (t1 - t0) * 20;
  while (__builtin_amdgcn_s_memrealtime() < until) __builtin_amdgcn_s_sleep(2);
  if (v == 0x12345) sink[0] = v;
}
extern "C" void kernel_launch(void* const* d_in, const int* in_sizes, int n_in, void* d_out, int out_size, void* d_ws,
                              size_t ws_size, hipStream_t stream) {
  const unsigned short* x = (const unsigned short*)d_in[0];
  const unsigned short* w1 = (const unsigned short*)d_in[1];
  const unsigned short* b1 = (const unsigned short*)d_in[2];
  const unsigned short* w2 = (const unsigned short*)d_in[3];
  const unsigned short* b2 = (const unsigned short*)d_in[4];
  unsigned short* hf = (unsigned short*)d_ws;
  unsigned short* gf = hf + (size_t)S * D;
  k_probe<8><<<256, 256, 0, stream>>>((unsigned*)d_ws);
  k_ln<<<S / 4, 256, 0, stream>>>(x, hf);
  k_gemm<S / 32, F, true><<<8 * 22, 256, 0, stream>>>(hf, w1, b1, gf);
  k_gemm<F / 32, S, false><<<8 * 32, 256, 0, stream>>>(gf, w2, b2, (unsigned short*)d_out);
}
```

```cpp
#include <hip/hip_runtime.h>
#include <hip/hip_bf16.h>
#include <stdint.h>

typedef short bf16x8 __attribute__((ext_vector_type(8)));
typedef short s16x4 __attribute__((ext_vector_type(4)));
typedef float f32x4 __attribute__((ext_vector_type(4)));
typedef unsigned u32x4 __attribute__((ext_vector_type(4)));
typedef unsigned u32x2 __attribute__((ext_vector_type(2)));

constexpr int S = 2048, D = 512, F = 1376;

__device__ __forceinline__ float bf2f(unsigned short b) { return __builtin_bit_cast(float, (unsigned)b << 16); }
__device__ __forceinline__ unsigned short f2bf(float f) { __bf16 b = (__bf16)f; return __builtin_bit_cast(unsigned short, b); }

__global__ __launch_bounds__(256) void k_ln(const unsigned short* __restrict__ x, unsigned short* __restrict__ hf) {
  int wave = threadIdx.x >> 6, lane = threadIdx.x & 63;
  int s = blockIdx.x * 4 + wave;
  u32x4 v = *(const u32x4*)(x + (size_t)s * D + lane * 8);
  float f[8];
  for (int i = 0; i < 4; ++i) { f[2*i] = __builtin_bit_cast(float, v[i] << 16); f[2*i+1] = __builtin_bit_cast(float, v[i] & 0xffff0000u); }
  float sum = 0; for (int i = 0; i < 8; ++i) sum += f[i];
  for (int o = 32; o; o >>= 1) sum += __shfl_xor(sum, o);
  float m = sum * (1.0f / D);
  float q = 0; for (int i = 0; i < 8; ++i) { float t = f[i] - m; q += t * t; }
  for (int o = 32; o; o >>= 1) q += __shfl_xor(q, o);
  float r = rsqrtf(q * (1.0f / D) + 1e-5f);
  int ks = s >> 5, kq = (s >> 3) & 3, j = s & 7;
  for (int i = 0; i < 8; ++i) {
    int d = lane * 8 + i;
    int dt = d >> 4, dr = d & 15;
    hf[(((size_t)dt * 64 + ks) * 64 + kq * 16 + dr) * 8 + j] = f2bf((f[i] - m) * r);
  }
}

__device__ __forceinline__ float gelu_t(float u) {
  float z = 0.7978845608028654f * (u + 0.044715f * u * u * u);
  float e = __expf(2.0f * z);
  float t = 1.0f - 2.0f / (e + 1.0f);
  return 0.5f * u * (1.0f + t);
}

template <int KSTEPS, int NB, bool FIRST>
__global__ __launch_bounds__(256) void k_gemm(const unsigned short* __restrict__ Af, const unsigned short* __restrict__ B,
                                              const unsigned short* __restrict__ bias, unsigned short* __restrict__ out) {
  __shared__ __attribute__((aligned(16))) unsigned short sb[4][32][64 + 8];
  __shared__ __attribute__((aligned(16))) float red[4][64][65];
  int wave = threadIdx.x >> 6, lane = threadIdx.x & 63;
  int mt = blockIdx.x & 7, nt = blockIdx.x >> 3;
  int n0 = nt * 64;
  f32x4 acc[4][4] = {};
  for (int ks = wave; ks < KSTEPS; ks += 4) {
    for (int i = 0; i < 4; ++i) {
      int r = i * 8 + (lane >> 3), c = (lane & 7) * 8;
      u32x4 v = {0,0,0,0};
      if (n0 + c < NB) v = *(const u32x4*)(B + (size_t)(ks * 32 + r) * NB + n0 + c);
      *(u32x4*)&sb[wave][r][c] = v;
    }
    bf16x8 a[4], b[4];
    for (int m = 0; m < 4; ++m) a[m] = *(const bf16x8*)(Af + (((size_t)(mt * 4 + m) * KSTEPS + ks) * 64 + lane) * 8);
    __builtin_amdgcn_s_waitcnt(0);
    __builtin_amdgcn_wave_barrier();
    for (int n = 0; n < 4; ++n)
      for (int j = 0; j < 8; ++j) b[n][j] = (short)sb[wave][(lane >> 4) * 8 + j][n * 16 + (lane & 15)];
    for (int m = 0; m < 4; ++m) for (int n = 0; n < 4; ++n)
      acc[m][n] = __builtin_amdgcn_mfma_f32_16x16x32_bf16(a[m], b[n], acc[m][n], 0, 0, 0);
    __builtin_amdgcn_wave_barrier();
  }
  for (int m = 0; m < 4; ++m) for (int n = 0; n < 4; ++n) for (int j = 0; j < 4; ++j)
    red[wave][m * 16 + (lane >> 4) * 4 + j][n * 16 + (lane & 15)] = acc[m][n][j];
  __syncthreads();
  for (int i = threadIdx.x; i < 64 * 64; i += 256) {
    if (FIRST) {
      int n = i & 63, m = i >> 6;
      int f = n0 + n; if (f >= NB) continue;
      float u = red[0][m][n] + red[1][m][n] + red[2][m][n] + red[3][m][n] + bf2f(bias[f]);
      float g = gelu_t(u);
      int d = mt * 64 + m;
      out[((((size_t)(d >> 4)) * (F / 32) + (f >> 5)) * 64 + ((f >> 3) & 3) * 16 + (d & 15)) * 8 + (f & 7)] = f2bf(g);
    } else {
      int m = i & 63, n = i >> 6;
      int s = n0 + n, d = mt * 64 + m;
      float u = red[0][m][n] + red[1][m][n] + red[2][m][n] + red[3][m][n] + bf2f(bias[s]);
      out[(size_t)s * D + d] = f2bf(u);
    }
  }
}


template <int N, int PF>
__global__ __launch_bounds__(256) void k_probe(unsigned* sink) {
  unsigned v = threadIdx.x, a = 1, b = 2, c = 3, d = 4;
  if (PF) {
    unsigned long long pc;
    asm volatile("s_getpc_b64 %0" : "=s"(pc));
    const char* p = (const char*)(pc & ~127ull) + threadIdx.x * 128;
    unsigned r = 0;
    if (threadIdx.x * 128 < N * 16 + 256) {
      asm volatile("global_load_dword %0, %1, off\n\ts_waitcnt vmcnt(0)" : "=&v"(r) : "v"(p) : "memory");
    }
    a += r & 0;
    __syncthreads();
  }
  unsigned long long t0 = __builtin_amdgcn_s_memrealtime();
  asm volatile("s_waitcnt lgkmcnt(0)");
  asm volatile(".rept %c5\n\tv_add3_u32 %0, %0, %2, %3\n\tv_add3_u32 %1, %1, %2, %4\n\t.endr" : "+v"(v), "+v"(d) : "v"(a), "v"(b), "v"(c), "i"(N));
  unsigned long long t1 = __builtin_amdgcn_s_memrealtime();
  unsigned long long until = t1 + (t1 - t0) * 20;
  while (__builtin_amdgcn_s_memrealtime() < until) __builtin_amdgcn_s_sleep(2);
  if (v + d == 0x12345) sink[0] = v;
}
extern "C" void kernel_launch(void* const* d_in, const int* in_sizes, int n_in, void* d_out, int out_size, void* d_ws,
                              size_t ws_size, hipStream_t stream) {
  const unsigned short* x = (const unsigned short*)d_in[0];
  const unsigned short* w1 = (const unsigned short*)d_in[1];
  const unsigned short* b1 = (const unsigned short*)d_in[2];
  const unsigned short* w2 = (const unsigned short*)d_in[3];
  const unsigned short* b2 = (const unsigned short*)d_in[4];
  unsigned short* hf = (unsigned short*)d_ws;
  unsigned short* gf = hf + (size_t)S * D;
  k_probe<4, 1><<<256, 256, 0, stream>>>((unsigned*)d_ws);
  k_ln<<<S / 4, 256, 0, stream>>>(x, hf);
  k_gemm<S / 32, F, true><<<8 * 22, 256, 0, stream>>>(hf, w1, b1, gf);
  k_gemm<F / 32, S, false><<<8 * 32, 256, 0, stream>>>(gf, w2, b2, (unsigned short*)d_out);
}
```

```cpp
#include <hip/hip_runtime.h>
#include <hip/hip_bf16.h>
#include <stdint.h>

#define PROFILE 0

typedef float f32x16 __attribute__((ext_vector_type(16)));
typedef float f32x2 __attribute__((ext_vector_type(2)));
typedef int i32x4 __attribute__((ext_vector_type(4)));
typedef unsigned u32x4 __attribute__((ext_vector_type(4)));
typedef unsigned u32x2 __attribute__((ext_vector_type(2)));
typedef __bf16 bf16x2 __attribute__((ext_vector_type(2)));
#define GAS __attribute__((address_space(1)))
#define LAS __attribute__((address_space(3)))

constexpr int S = 2048, D = 512, F = 1376;
constexpr int P1 = F * 2, P2 = S * 2;
constexpr int WS_E = 0, WS_R = 4096, WS_HF = 8192, WS_GF = 12288, WS_CTRL = 32768;
constexpr int WS_DBG = 65536;
constexpr int WS_H = 1 << 20, H_TILE = 128 * 2048;
constexpr int WS_G = 4 << 20, G_TILE = 86 * 2048;
constexpr int L_XCH = 0, L_OUT = 49152, L_RING = 65536, RING_W = 24576, LDS_BYTES = 163840;
constexpr int NHELP = 10;
constexpr unsigned SPIN_CAP = 1u << 20;

#define ACLOB10(p) "a" #p "0", "a" #p "1", "a" #p "2", "a" #p "3", "a" #p "4", "a" #p "5", "a" #p "6", "a" #p "7", "a" #p "8", "a" #p "9"
#define ACLOB_ALL "a0","a1","a2","a3","a4","a5","a6","a7","a8","a9", ACLOB10(1), ACLOB10(2), ACLOB10(3), ACLOB10(4), ACLOB10(5), ACLOB10(6), ACLOB10(7), ACLOB10(8), ACLOB10(9), \
  ACLOB10(10), ACLOB10(11), ACLOB10(12), ACLOB10(13), ACLOB10(14), ACLOB10(15), ACLOB10(16), ACLOB10(17), ACLOB10(18), ACLOB10(19), ACLOB10(20), ACLOB10(21), ACLOB10(22), ACLOB10(23), ACLOB10(24), \
  "a250","a251","a252","a253","a254","a255"
#define VCLOB10(p) "v" #p "0", "v" #p "1", "v" #p "2", "v" #p "3", "v" #p "4", "v" #p "5", "v" #p "6", "v" #p "7", "v" #p "8", "v" #p "9"
#define VCLOB_ALL VCLOB10(8), VCLOB10(9), VCLOB10(10), VCLOB10(11), VCLOB10(12), VCLOB10(13), VCLOB10(14), VCLOB10(15), VCLOB10(16), VCLOB10(17), VCLOB10(18), VCLOB10(19), VCLOB10(20)

__device__ __forceinline__ i32x4 make_srd(const void* p, unsigned bytes) {
  const uint64_t a = (uint64_t)p;
  return i32x4{(int)(uint32_t)a, (int)(uint32_t)(a >> 32), (int)bytes, 0x00020000};
}
__device__ __forceinline__ float asf(unsigned u) { return __builtin_bit_cast(float, u); }
__device__ __forceinline__ unsigned asu(float f) { return __builtin_bit_cast(unsigned, f); }
__device__ __forceinline__ unsigned pk_bf16(float lo, float hi) {
  bf16x2 v = {(__bf16)lo, (__bf16)hi};
  return __builtin_bit_cast(unsigned, v);
}
template <int CTRL> __device__ __forceinline__ float dpp_add(float v) {
  return v + asf((unsigned)__builtin_amdgcn_update_dpp(0, (int)asu(v), CTRL, 0xf, 0xf, true));
}
__device__ __forceinline__ void reduce8(const float (&v)[8], float (&r)[2]) {
  float a[4];
#pragma unroll
  for (int i = 0; i < 4; ++i) {
    auto t = __builtin_amdgcn_permlane32_swap(asu(v[i]), asu(v[i + 4]), false, false);
    const unsigned t0 = t[0], t1 = t[1];
    a[i] = asf(t0) + asf(t1);
  }
#pragma unroll
  for (int i = 0; i < 2; ++i) {
    auto t = __builtin_amdgcn_permlane16_swap(asu(a[i]), asu(a[i + 2]), false, false);
    const unsigned t0 = t[0], t1 = t[1];
    float s = asf(t0) + asf(t1);
    s = dpp_add<0x128>(s); s = dpp_add<0x124>(s); s = dpp_add<0x122>(s); s = dpp_add<0x121>(s);
    r[i] = s;
  }
}
__device__ __forceinline__ float bcast(const float (&r)[2], int row) {
  return asf((unsigned)__builtin_amdgcn_readlane((int)asu(r[row & 1]), (row >> 1) * 16));
}

#define STAMP_ASM(n) ""
#define STAMP(i) do {} while (0)

#define DMA_GROUP(V0, V1, V2, V3, SRD, SO, STEP, END, START) \
  "s_add_u32 m0, %[ring], slot*4096\n" \
  "s_nop 0\n" \
  "buffer_load_dwordx4 " V0 ", " SRD ", " SO " offen lds\n" \
  "buffer_load_dwordx4 " V1 ", " SRD ", " SO " offen offset:1024 lds\n" \
  "buffer_load_dwordx4 " V2 ", " SRD ", " SO " offen offset:2048 lds\n" \
  "buffer_load_dwordx4 " V3 ", " SRD ", " SO " offen offset:3072 lds\n" \
  "s_add_u32 " SO ", " SO ", " STEP "\n" \
  "s_cmp_eq_u32 " SO ", " END "\n" \
  "s_cselect_b32 " SO ", " START ", " SO "\n"
#define DMA_W1 DMA_GROUP("%[vw0]", "%[vw1]", "%[vw2]", "%[vw3]", "%[srdw]", "%[sow]", "32*2752", "%[sowe]", "%[sows]")
#define DMA_W2 DMA_GROUP("%[vx0]", "%[vx1]", "%[vx2]", "%[vx3]", "%[srdx]", "%[sox]", "32*4096", "%[soxe]", "%[soxs]")
#define TR_GROUP(TOFF) \
  "ds_read_b64_tr_b16 a[16*g+0:16*g+1], %[tr0] offset:(g%%6)*4096\n" \
  "ds_read_b64_tr_b16 a[16*g+2:16*g+3], %[tr0] offset:(g%%6)*4096+" TOFF "\n" \
  "ds_read_b64_tr_b16 a[16*g+4:16*g+5], %[tr1] offset:(g%%6)*4096\n" \
  "ds_read_b64_tr_b16 a[16*g+6:16*g+7], %[tr1] offset:(g%%6)*4096+" TOFF "\n" \
  "ds_read_b64_tr_b16 a[16*g+8:16*g+9], %[tr0] offset:(g%%6)*4096+2048\n" \
  "ds_read_b64_tr_b16 a[16*g+10:16*g+11], %[tr0] offset:(g%%6)*4096+2048+" TOFF "\n" \
  "ds_read_b64_tr_b16 a[16*g+12:16*g+13], %[tr1] offset:(g%%6)*4096+2048\n" \
  "ds_read_b64_tr_b16 a[16*g+14:16*g+15], %[tr1] offset:(g%%6)*4096+2048+" TOFF "\n"
#define ACT_LOAD(ST) \
  "buffer_load_dwordx4 v[128+16*sb+8*" ST ":131+16*sb+8*" ST "], %[va], %[srda], %[soa] offen offset:" ST "*2048 sc1\n" \
  "buffer_load_dwordx4 v[132+16*sb+8*" ST ":135+16*sb+8*" ST "], %[vb], %[srda], %[soa] offen offset:" ST "*2048 sc1\n"
#define ACT_STEP \
  "s_add_u32 %[soa], %[soa], 4096\n" \
  "s_cmp_eq_u32 %[soa], %[soae]\n" \
  "s_cselect_b32 %[soa], %[soas], %[soa]\n"
#define POLL(L) \
  "s_mov_b32 %[cnt], 0x100000\n" \
  L "0%=:\n" \
  "buffer_load_dword v192, %[vfl], %[srdc], 0 offen sc1\n" \
  "s_waitcnt vmcnt(0)\n" \
  "v_cmp_ne_u32 vcc, %[tag], v192\n" \
  "s_and_b64 vcc, vcc, %[pmask]\n" \
  "s_cbranch_scc0 " L "1%=\n" \
  "s_sub_u32 %[cnt], %[cnt], 1\n" \
  "s_cbranch_scc0 " L "0%=\n" \
  L "1%=:\n"
#define XCHG \
  "s_nop 7\n s_nop 7\n" \
  ".set r, 0\n .rept 4\n ds_write_b128 %[xw1], v[80+4*r:83+4*r] offset:r*1024\n .set r, r+1\n .endr\n" \
  ".set r, 0\n .rept 4\n ds_write_b128 %[xw2], v[96+4*r:99+4*r] offset:4096+r*1024\n .set r, r+1\n .endr\n" \
  ".set r, 0\n .rept 4\n ds_write_b128 %[xw3], v[112+4*r:115+4*r] offset:8192+r*1024\n .set r, r+1\n .endr\n" \
  "s_waitcnt lgkmcnt(0)\n" \
  "s_barrier\n" \
  ".set r, 0\n .rept 12\n ds_read_b128 v[80+4*r:83+4*r], %[xr] offset:r*1024\n .set r, r+1\n .endr\n" \
  "s_waitcnt lgkmcnt(8)\n" \
  ".set r, 0\n .rept 8\n v_pk_add_f32 v[64+2*r:65+2*r], v[64+2*r:65+2*r], v[80+2*r:81+2*r]\n .set r, r+1\n .endr\n" \
  "s_waitcnt lgkmcnt(4)\n" \
  ".set r, 0\n .rept 8\n v_pk_add_f32 v[64+2*r:65+2*r], v[64+2*r:65+2*r], v[96+2*r:97+2*r]\n .set r, r+1\n .endr\n" \
  "s_waitcnt lgkmcnt(0)\n" \
  ".set r, 0\n .rept 8\n v_pk_add_f32 v[64+2*r:65+2*r], v[64+2*r:65+2*r], v[112+2*r:113+2*r]\n .set r, r+1\n .endr\n"

__global__ __launch_bounds__(256, 1) void mixer_kernel(const unsigned short* __restrict__ x, const unsigned short* __restrict__ w1,
                                                       const unsigned short* __restrict__ b1, const unsigned short* __restrict__ w2,
                                                       const unsigned short* __restrict__ b2, unsigned short* __restrict__ out,
                                                       unsigned char* __restrict__ ws) {
  extern __shared__ __attribute__((aligned(16))) unsigned char lds[];
  STAMP(0);
  const int lane = threadIdx.x & 63;
  const int w = __builtin_amdgcn_readfirstlane(threadIdx.x >> 6);
  const int b = blockIdx.x, xc = b & 7, j = b >> 3, half = xc & 1, q = xc >> 1;
  const unsigned ldsb = (unsigned)(uintptr_t)(LAS unsigned char*)lds;
  const i32x4 srdc = make_srd(ws, WS_CTRL);
  const __amdgpu_buffer_rsrc_t rsc = __builtin_amdgcn_make_buffer_rsrc(ws, 0, 8 << 20, 0x00020000);

  const int hh = lane >> 5, blk = (lane >> 4) & 1, tq = (lane & 15) >> 2, tp = lane & 3;
  const unsigned ring = ldsb + L_RING + w * RING_W;
  const unsigned trc = ring + 32 * blk + 8 * tp;
  const unsigned sw0 = 64 * ((w >> 1) ^ (tq >> 1)), sw1 = sw0 ^ 64;
  const unsigned va = lane * 16 + (w & 1) * 1024, vb = va ^ 1024;
  const unsigned chunk = ((lane & 7) ^ (((lane >> 4) & 1) << 2)) * 16;
  const unsigned xw1 = ldsb + L_XCH + (w ^ 1) * 12288 + lane * 16, xw2 = ldsb + L_XCH + (w ^ 2) * 12288 + lane * 16,
                 xw3 = ldsb + L_XCH + (w ^ 3) * 12288 + lane * 16, xr = ldsb + L_XCH + w * 12288 + lane * 16;
  const int st = q * 8 + (j & 7), dt2 = half * 4 + (j >> 3);
  const i32x4 srdx = make_srd(w2 + st * 64, (unsigned)(F * P2 - st * 128));
  const unsigned vx0 = (lane >> 3) * P2 + chunk, vx1 = vx0 + 8 * P2 - 1024, vx2 = vx0 + 16 * P2 - 2048, vx3 = vx0 + 24 * P2 - 3072;
  const int rot2 = ((j >> 3) & 1) ^ (j & 1) ? 5 : 0;
  const unsigned soxs = (unsigned)w * (352 * P2), soxe = soxs + 352 * P2;
  unsigned sox = soxs + rot2 * (32 * P2);
  unsigned tag;

  if (j < NHELP) {
    if (j < 8) {
      const int u = (xc * 8 + j) * 4 + w;
      u32x4 xv[8];
#pragma unroll
      for (int i = 0; i < 8; ++i) xv[i] = *(const GAS u32x4*)((const GAS unsigned char*)x + (size_t)(8 * u + i) * 1024 + lane * 16);
      const unsigned e = __builtin_amdgcn_raw_buffer_load_b32(rsc, WS_E, 0, 16);
      float s8[8];
      const bf16x2 ones = {(__bf16)1.0f, (__bf16)1.0f};
#pragma unroll
      for (int i = 0; i < 8; ++i) {
        float s = 0.f;
#pragma unroll
        for (int c = 0; c < 4; ++c) { const unsigned t = xv[i][c]; s = __builtin_amdgcn_fdot2_f32_bf16(__builtin_bit_cast(bf16x2, t), ones, s, false); }
        s8[i] = s;
      }
      STAMP(1);
      float rm[2];
      reduce8(s8, rm);
      rm[0] *= (1.0f / D); rm[1] *= (1.0f / D);
      f32x2 dv[4][8];
      float q8[8];
#pragma unroll
      for (int p = 0; p < 4; ++p) {
        const f32x2 m = {bcast(rm, 2 * p), bcast(rm, 2 * p + 1)};
        f32x2 qq = {0.f, 0.f};
#pragma unroll
        for (int c = 0; c < 8; ++c) {
          const unsigned t0 = xv[2 * p][c >> 1], t1 = xv[2 * p + 1][c >> 1];
          f32x2 v = (c & 1) ? f32x2{asf(t0 & 0xffff0000u), asf(t1 & 0xffff0000u)} : f32x2{asf(t0 << 16), asf(t1 << 16)};
          v -= m;
          dv[p][c] = v;
          qq += v * v;
        }
        q8[2 * p] = qq[0]; q8[2 * p + 1] = qq[1];
      }
      float rv[2];
      reduce8(q8, rv);
      rv[0] = __builtin_amdgcn_rsqf(rv[0] * (1.0f / D) + 1e-5f);
      rv[1] = __builtin_amdgcn_rsqf(rv[1] * (1.0f / D) + 1e-5f);
      f32x2 rs[4];
#pragma unroll
      for (int p = 0; p < 4; ++p) rs[p] = f32x2{bcast(rv, 2 * p), bcast(rv, 2 * p + 1)};
      const unsigned ho = WS_H + (lane >> 3) * H_TILE + (u >> 1) * 2048 + (u & 1) * 512 + (lane & 7) * 16;
#pragma unroll
      for (int c = 0; c < 8; ++c) {
        u32x4 o;
#pragma unroll
        for (int p = 0; p < 4; ++p) { const f32x2 v = dv[p][c] * rs[p]; o[p] = pk_bf16(v[0], v[1]); }
        __builtin_amdgcn_raw_buffer_store_b128(o, rsc, ho + (c >> 2) * 1024 + (c & 3) * 128, 0, 16);
      }
      STAMP(2);
      tag = __builtin_amdgcn_readfirstlane(e) * 0x9E3779B1u + 0x7F4A7C15u;
      asm volatile("s_waitcnt vmcnt(0)" ::: "memory");
      if (lane == 0) __builtin_amdgcn_raw_buffer_store_b32(tag, rsc, WS_HF + u * 4, 0, 16);
      STAMP(3);
    } else {
      const unsigned e = __builtin_amdgcn_raw_buffer_load_b32(rsc, WS_E, 0, 16);
      tag = __builtin_amdgcn_readfirstlane(e) * 0x9E3779B1u + 0x7F4A7C15u;
    }
    if (lane == 0) __builtin_amdgcn_raw_buffer_store_b32(tag, rsc, WS_R + (b * 4 + w) * 4, 0, 16);
    if (b == 9 * 8) {
      for (unsigned it = 0; it < SPIN_CAP; ++it) {
        const u32x4 v = __builtin_amdgcn_raw_buffer_load_b128(rsc, WS_R + (w * 64 + lane) * 16, 0, 16);
        if (__all(v[0] == tag && v[1] == tag && v[2] == tag && v[3] == tag)) break;
        __builtin_amdgcn_s_sleep(4);
      }
      __builtin_amdgcn_s_barrier();
      if (threadIdx.x == 0) __builtin_amdgcn_raw_buffer_store_b32(tag, rsc, WS_E, 0, 16);
    }
    asm volatile(
        ".set slot, 0\n .rept 6\n" DMA_W2 ".set slot, slot+1\n .endr\n"
        : [sox] "+s"(sox)
        : [ring] "s"(ring), [vx0] "v"(vx0), [vx1] "v"(vx1), [vx2] "v"(vx2), [vx3] "v"(vx3), [srdx] "s"(srdx), [soxe] "s"(soxe), [soxs] "s"(soxs)
        : "memory", "scc");
  } else {
    const int idx = q * 22 + (j - NHELP), ft = idx >> 2, dt = half * 4 + (idx & 3);
    const int f0 = ft < 21 ? ft * 64 : F - 64;
    const i32x4 srdw = make_srd(w1 + f0, (unsigned)(S * P1 - f0 * 2));
    const i32x4 srda = make_srd(ws + WS_H + dt * H_TILE, H_TILE);
    const i32x4 srdb = make_srd(b1 + f0, 128);
    const unsigned vw0 = (lane >> 3) * P1 + chunk, vw1 = vw0 + 8 * P1 - 1024, vw2 = vw0 + 16 * P1 - 2048, vw3 = vw0 + 24 * P1 - 3072;
    const int rot = (((idx & 3) + ft) & 3) * 4 + half * 2;
    const unsigned sows = (unsigned)w * (512 * P1), sowe = sows + 512 * P1;
    unsigned sow = sows + rot * (32 * P1);
    const unsigned soas = (unsigned)w * 65536u, soae = soas + 65536u;
    unsigned soa = soas + rot * 4096;
    const unsigned tr0 = trc + (8 * hh + tq) * 128 + sw0, tr1 = trc + (8 * hh + tq) * 128 + sw1;
    const unsigned vfl = WS_HF + (w * 64 + lane) * 4;
    const unsigned vbias = (w >> 1) * 64 + hh * 8;
    const unsigned vr = WS_R + (b * 4 + w) * 4;
    const unsigned long long pmask = ~0ull;
    unsigned cnt;
    f32x16 acc;
    asm volatile(
        "buffer_load_dword v193, off, %[srdc], 0 sc1\n"
        "buffer_load_dwordx2 v[194:195], %[vbias], %[srdb], 0 offen\n"
        "buffer_load_dwordx2 v[196:197], %[vbias], %[srdb], 0 offen offset:16\n"
        "buffer_load_dwordx2 v[198:199], %[vbias], %[srdb], 0 offen offset:32\n"
        "buffer_load_dwordx2 v[200:201], %[vbias], %[srdb], 0 offen offset:48\n"
        ".set slot, 0\n .rept 5\n" DMA_W1 ".set slot, slot+1\n .endr\n"
        "s_waitcnt vmcnt(20)\n"
        "v_readfirstlane_b32 %[tag], v193\n"
        ".set r, 0\n .rept 8\n v_lshlrev_b32 v[64+2*r], 16, v[194+r]\n v_and_b32 v[65+2*r], 0xffff0000, v[194+r]\n .set r, r+1\n .endr\n"
        "s_mul_i32 %[tag], %[tag], 0x9E3779B1\n"
        "s_add_u32 %[tag], %[tag], 0x7F4A7C15\n"
        "v_mov_b32 v193, %[tag]\n"
        "buffer_store_dword v193, %[vr], %[srdc], 0 offen sc1\n"
        ".set g, 0\n .rept 11\n"
        "s_waitcnt vmcnt(16)\n"
        TR_GROUP("512")
        "s_waitcnt lgkmcnt(8)\n"
        ".set slot, (g+5)%%6\n"
        DMA_W1
        ".set g, g+1\n .endr\n"
        STAMP_ASM(1)
        POLL(".Lp")
        STAMP_ASM(2)
        ".set sb, 0\n .rept 4\n" ACT_LOAD("0") ACT_LOAD("1") ACT_STEP ".set sb, sb+1\n .endr\n"
        ".rept 5\n"
        TR_GROUP("512")
        "s_waitcnt lgkmcnt(8)\n"
        ".set g, g+1\n .endr\n"
        "s_waitcnt lgkmcnt(0)\n"
        ".set g, 0\n .rept 16\n"
        ".set sb, g%%4\n"
        "s_waitcnt vmcnt(14)\n"
        ".if g == 0\n"
        "v_mfma_f32_32x32x16_bf16 v[64:79], a[16*g:16*g+3], v[128+16*sb:131+16*sb], v[64:79]\n"
        "v_mfma_f32_32x32x16_bf16 v[80:95], a[16*g:16*g+3], v[132+16*sb:135+16*sb], 0\n"
        "v_mfma_f32_32x32x16_bf16 v[96:111], a[16*g+4:16*g+7], v[128+16*sb:131+16*sb], 0\n"
        "v_mfma_f32_32x32x16_bf16 v[112:127], a[16*g+4:16*g+7], v[132+16*sb:135+16*sb], 0\n"
        ".else\n"
        "v_mfma_f32_32x32x16_bf16 v[64:79], a[16*g:16*g+3], v[128+16*sb:131+16*sb], v[64:79]\n"
        "v_mfma_f32_32x32x16_bf16 v[80:95], a[16*g:16*g+3], v[132+16*sb:135+16*sb], v[80:95]\n"
        "v_mfma_f32_32x32x16_bf16 v[96:111], a[16*g+4:16*g+7], v[128+16*sb:131+16*sb], v[96:111]\n"
        "v_mfma_f32_32x32x16_bf16 v[112:127], a[16*g+4:16*g+7], v[132+16*sb:135+16*sb], v[112:127]\n"
        ".endif\n"
        ".if g < 12\n" ACT_LOAD("0") "s_waitcnt vmcnt(14)\n" ".else\n" "s_waitcnt vmcnt(12)\n" ".endif\n"
        "v_mfma_f32_32x32x16_bf16 v[80:95], a[16*g+8:16*g+11], v[140+16*sb:143+16*sb], v[80:95]\n"
        "v_mfma_f32_32x32x16_bf16 v[96:111], a[16*g+12:16*g+15], v[136+16*sb:139+16*sb], v[96:111]\n"
        "v_mfma_f32_32x32x16_bf16 v[112:127], a[16*g+12:16*g+15], v[140+16*sb:143+16*sb], v[112:127]\n"
        "v_mfma_f32_32x32x16_bf16 v[64:79], a[16*g+8:16*g+11], v[136+16*sb:139+16*sb], v[64:79]\n"
        ".if g < 12\n" ACT_LOAD("1") ACT_STEP ".else\n" ".set slot, g-12\n" DMA_W2 ".endif\n"
        ".set g, g+1\n .endr\n"
        ".set slot, 4\n" DMA_W2 ".set slot, 5\n" DMA_W2
        STAMP_ASM(3)
        XCHG
        : "=&{v[64:79]}"(acc), [sow] "+s"(sow), [soa] "+s"(soa), [sox] "+s"(sox), [cnt] "=&s"(cnt), [tag] "=&s"(tag)
        : [ring] "s"(ring), [vw0] "v"(vw0), [vw1] "v"(vw1), [vw2] "v"(vw2), [vw3] "v"(vw3), [srdw] "s"(srdw), [sowe] "s"(sowe), [sows] "s"(sows),
          [vx0] "v"(vx0), [vx1] "v"(vx1), [vx2] "v"(vx2), [vx3] "v"(vx3), [srdx] "s"(srdx), [soxe] "s"(soxe), [soxs] "s"(soxs),
          [tr0] "v"(tr0), [tr1] "v"(tr1), [va] "v"(va), [vb] "v"(vb), [srda] "s"(srda), [soae] "s"(soae), [soas] "s"(soas),
          [vfl] "v"(vfl), [srdc] "s"(srdc), [pmask] "s"(pmask), [vbias] "v"(vbias), [srdb] "s"(srdb), [vr] "v"(vr),
          [xw1] "v"(xw1), [xw2] "v"(xw2), [xw3] "v"(xw3), [xr] "v"(xr)
        : "memory", "scc", "vcc", VCLOB_ALL, ACLOB_ALL);
    STAMP(4);
    u32x4 o[2];
#pragma unroll
    for (int r = 0; r < 8; ++r) {
      const f32x2 u = {acc[2 * r], acc[2 * r + 1]};
      constexpr float k0 = -2.0f * 0.7978845608028654f * 1.4426950408889634f, k1 = k0 * 0.044715f;
      const f32x2 t = u * (u * u * k1 + k0);
      const f32x2 dn = f32x2{__builtin_amdgcn_exp2f(t[0]), __builtin_amdgcn_exp2f(t[1])} + 1.0f;
      const f32x2 g = u * f32x2{__builtin_amdgcn_rcpf(dn[0]), __builtin_amdgcn_rcpf(dn[1])};
      o[r >> 2][r & 3] = pk_bf16(g[0], g[1]);
    }
    const int f32t = (f0 >> 5) + (w >> 1);
    if (ft < 21 || w >= 2) {
      const unsigned go = WS_G + dt * G_TILE + f32t * 4096 + (w & 1) * 1024 + lane * 16;
      __builtin_amdgcn_raw_buffer_store_b128(o[0], rsc, go, 0, 16);
      __builtin_amdgcn_raw_buffer_store_b128(o[1], rsc, go + 2048, 0, 16);
      STAMP(5);
      asm volatile("s_waitcnt vmcnt(0)" ::: "memory");
      if (lane == 0) __builtin_amdgcn_raw_buffer_store_b32(tag, rsc, WS_GF + (dt * 128 + f32t * 2 + (w & 1)) * 4, 0, 16);
    }
  }
  STAMP(6);
  {
    const i32x4 srda = make_srd(ws + WS_G + dt2 * G_TILE, G_TILE);
    const i32x4 srdb = make_srd(b2 + st * 64, 128);
    const unsigned soas = (unsigned)w * (11 * 4096), soae = soas + 11 * 4096;
    unsigned soa = soas + rot2 * 4096;
    const unsigned tr0 = trc + (4 * hh + tq) * 128 + sw0, tr1 = trc + (4 * hh + tq) * 128 + sw1;
    const unsigned vfl = WS_GF + (dt2 * 128 + w * 22 + lane) * 4;
    const unsigned long long pmask = w < 3 ? (1ull << 22) - 1 : (1ull << 20) - 1;
    const unsigned vbias = (w >> 1) * 64 + (lane & 31) * 2;
    unsigned cnt;
    f32x16 acc;
    asm volatile(
        "buffer_load_ushort v194, %[vbias], %[srdb], 0 offen\n"
        ".set g, 0\n .rept 6\n"
        ".if g == 0\n s_waitcnt vmcnt(21)\n .else\n s_waitcnt vmcnt(16)\n .endif\n"
        TR_GROUP("1024")
        ".if g > 0\n"
        "s_waitcnt lgkmcnt(8)\n"
        ".set slot, (g+5)%%6\n"
        DMA_W2
        ".endif\n"
        ".set g, g+1\n .endr\n"
        "s_barrier\n"
        STAMP_ASM(7)
        POLL(".Lq")
        STAMP_ASM(8)
        ".set sb, 0\n .rept 4\n" ACT_LOAD("0") ACT_LOAD("1") ACT_STEP ".set sb, sb+1\n .endr\n"
        "v_lshlrev_b32 v194, 16, v194\n"
        ".rept 5\n"
        TR_GROUP("1024")
        "s_waitcnt lgkmcnt(8)\n"
        ".set g, g+1\n .endr\n"
        ".set r, 0\n .rept 16\n v_mov_b32 v[64+r], v194\n .set r, r+1\n .endr\n"
        "s_waitcnt lgkmcnt(0)\n"
        ".set g, 0\n .rept 11\n"
        ".set sb, g%%4\n"
        ".if g < 8\n s_waitcnt vmcnt(14)\n .else\n s_waitcnt vmcnt(4*(10-g)+2)\n .endif\n"
        ".if g == 0\n"
        "v_mfma_f32_32x32x16_bf16 v[64:79], v[128+16*sb:131+16*sb], a[16*g:16*g+3], v[64:79]\n"
        "v_mfma_f32_32x32x16_bf16 v[80:95], v[132+16*sb:135+16*sb], a[16*g:16*g+3], 0\n"
        "v_mfma_f32_32x32x16_bf16 v[96:111], v[128+16*sb:131+16*sb], a[16*g+4:16*g+7], 0\n"
        "v_mfma_f32_32x32x16_bf16 v[112:127], v[132+16*sb:135+16*sb], a[16*g+4:16*g+7], 0\n"
        ".else\n"
        "v_mfma_f32_32x32x16_bf16 v[64:79], v[128+16*sb:131+16*sb], a[16*g:16*g+3], v[64:79]\n"
        "v_mfma_f32_32x32x16_bf16 v[80:95], v[132+16*sb:135+16*sb], a[16*g:16*g+3], v[80:95]\n"
        "v_mfma_f32_32x32x16_bf16 v[96:111], v[128+16*sb:131+16*sb], a[16*g+4:16*g+7], v[96:111]\n"
        "v_mfma_f32_32x32x16_bf16 v[112:127], v[132+16*sb:135+16*sb], a[16*g+4:16*g+7], v[112:127]\n"
        ".endif\n"
        ".if g < 7\n" ACT_LOAD("0") "s_waitcnt vmcnt(14)\n" ".else\n" "s_waitcnt vmcnt(4*(10-g))\n" ".endif\n"
        "v_mfma_f32_32x32x16_bf16 v[80:95], v[140+16*sb:143+16*sb], a[16*g+8:16*g+11], v[80:95]\n"
        "v_mfma_f32_32x32x16_bf16 v[96:111], v[136+16*sb:139+16*sb], a[16*g+12:16*g+15], v[96:111]\n"
        "v_mfma_f32_32x32x16_bf16 v[112:127], v[140+16*sb:143+16*sb], a[16*g+12:16*g+15], v[112:127]\n"
        "v_mfma_f32_32x32x16_bf16 v[64:79], v[136+16*sb:139+16*sb], a[16*g+8:16*g+11], v[64:79]\n"
        ".if g < 7\n" ACT_LOAD("1") ACT_STEP ".endif\n"
        ".set g, g+1\n .endr\n"
        STAMP_ASM(9)
        XCHG
        : "=&{v[64:79]}"(acc), [soa] "+s"(soa), [sox] "+s"(sox), [cnt] "=&s"(cnt)
        : [ring] "s"(ring), [vx0] "v"(vx0), [vx1] "v"(vx1), [vx2] "v"(vx2), [vx3] "v"(vx3), [srdx] "s"(srdx), [soxe] "s"(soxe), [soxs] "s"(soxs),
          [tr0] "v"(tr0), [tr1] "v"(tr1), [va] "v"(va), [vb] "v"(vb), [srda] "s"(srda), [soae] "s"(soae), [soas] "s"(soas),
          [vfl] "v"(vfl), [srdc] "s"(srdc), [pmask] "s"(pmask), [vbias] "v"(vbias), [srdb] "s"(srdb), [tag] "s"(tag),
          [xw1] "v"(xw1), [xw2] "v"(xw2), [xw3] "v"(xw3), [xr] "v"(xr)
        : "memory", "scc", "vcc", VCLOB_ALL, ACLOB_ALL);
    STAMP(10);
    LAS unsigned char* ot = (LAS unsigned char*)lds + L_OUT;
    {
      const unsigned o = ((w >> 1) * 32 + (lane & 31)) * 144 + (32 * hh + 4 * (w & 1)) * 2;
#pragma unroll
      for (int e = 0; e < 4; ++e)
        *(LAS u32x2*)(ot + o + e * 16) = u32x2{pk_bf16(acc[e], acc[4 + e]), pk_bf16(acc[8 + e], acc[12 + e])};
    }
    __syncthreads();
#pragma unroll
    for (int i = 0; i < 2; ++i) {
      const int row = w * 16 + i * 8 + (lane >> 3);
      const u32x4 v = *(LAS u32x4*)(ot + row * 144 + (lane & 7) * 16);
      *(GAS u32x4*)((GAS unsigned char*)out + (size_t)(st * 64 + row) * 1024 + dt2 * 128 + (lane & 7) * 16) = v;
    }
  }
}

extern "C" void kernel_launch(void* const* d_in, const int* in_sizes, int n_in, void* d_out, int out_size, void* d_ws,
                              size_t ws_size, hipStream_t stream) {
  (void)hipFuncSetAttribute((const void*)mixer_kernel, hipFuncAttributeMaxDynamicSharedMemorySize, LDS_BYTES);
  mixer_kernel<<<256, 256, LDS_BYTES, stream>>>((const unsigned short*)d_in[0], (const unsigned short*)d_in[1],
                                                (const unsigned short*)d_in[2], (const unsigned short*)d_in[3],
                                                (const unsigned short*)d_in[4], (unsigned short*)d_out, (unsigned char*)d_ws);
}
```

```cpp
#define POSTF 0
#define PREF 0
#define AMPF 0
#include <hip/hip_runtime.h>
#include <hip/hip_bf16.h>
#include <stdint.h>

#define PROFILE 0

typedef float f32x16 __attribute__((ext_vector_type(16)));
typedef float f32x2 __attribute__((ext_vector_type(2)));
typedef int i32x4 __attribute__((ext_vector_type(4)));
typedef unsigned u32x4 __attribute__((ext_vector_type(4)));
typedef unsigned u32x2 __attribute__((ext_vector_type(2)));
typedef __bf16 bf16x2 __attribute__((ext_vector_type(2)));
#define GAS __attribute__((address_space(1)))
#define LAS __attribute__((address_space(3)))

constexpr int S = 2048, D = 512, F = 1376;
constexpr int P1 = F * 2, P2 = S * 2;
constexpr int WS_E = 0, WS_R = 4096, WS_HF = 8192, WS_GF = 12288, WS_CTRL = 32768;
constexpr int WS_DBG = 65536;
constexpr int WS_H = 1 << 20, H_TILE = 128 * 2048;
constexpr int WS_G = 4 << 20, G_TILE = 86 * 2048;
constexpr int L_XCH = 0, L_OUT = 49152, L_RING = 65536, RING_W = 24576, LDS_BYTES = 163840;
constexpr int NHELP = 10;
constexpr unsigned SPIN_CAP = 1u << 20;

#define ACLOB10(p) "a" #p "0", "a" #p "1", "a" #p "2", "a" #p "3", "a" #p "4", "a" #p "5", "a" #p "6", "a" #p "7", "a" #p "8", "a" #p "9"
#define ACLOB_ALL "a0","a1","a2","a3","a4","a5","a6","a7","a8","a9", ACLOB10(1), ACLOB10(2), ACLOB10(3), ACLOB10(4), ACLOB10(5), ACLOB10(6), ACLOB10(7), ACLOB10(8), ACLOB10(9), \
  ACLOB10(10), ACLOB10(11), ACLOB10(12), ACLOB10(13), ACLOB10(14), ACLOB10(15), ACLOB10(16), ACLOB10(17), ACLOB10(18), ACLOB10(19), ACLOB10(20), ACLOB10(21), ACLOB10(22), ACLOB10(23), ACLOB10(24), \
  "a250","a251","a252","a253","a254","a255"
#define VCLOB10(p) "v" #p "0", "v" #p "1", "v" #p "2", "v" #p "3", "v" #p "4", "v" #p "5", "v" #p "6", "v" #p "7", "v" #p "8", "v" #p "9"
#define VCLOB_ALL VCLOB10(8), VCLOB10(9), VCLOB10(10), VCLOB10(11), VCLOB10(12), VCLOB10(13), VCLOB10(14), VCLOB10(15), VCLOB10(16), VCLOB10(17), VCLOB10(18), VCLOB10(19), VCLOB10(20)

__device__ __forceinline__ i32x4 make_srd(const void* p, unsigned bytes) {
  const uint64_t a = (uint64_t)p;
  return i32x4{(int)(uint32_t)a, (int)(uint32_t)(a >> 32), (int)bytes, 0x00020000};
}
__device__ __forceinline__ float asf(unsigned u) { return __builtin_bit_cast(float, u); }
__device__ __forceinline__ unsigned asu(float f) { return __builtin_bit_cast(unsigned, f); }
__device__ __forceinline__ unsigned pk_bf16(float lo, float hi) {
  bf16x2 v = {(__bf16)lo, (__bf16)hi};
  return __builtin_bit_cast(unsigned, v);
}
template <int CTRL> __device__ __forceinline__ float dpp_add(float v) {
  return v + asf((unsigned)__builtin_amdgcn_update_dpp(0, (int)asu(v), CTRL, 0xf, 0xf, true));
}
__device__ __forceinline__ void reduce8(const float (&v)[8], float (&r)[2]) {
  float a[4];
#pragma unroll
  for (int i = 0; i < 4; ++i) {
    auto t = __builtin_amdgcn_permlane32_swap(asu(v[i]), asu(v[i + 4]), false, false);
    const unsigned t0 = t[0], t1 = t[1];
    a[i] = asf(t0) + asf(t1);
  }
#pragma unroll
  for (int i = 0; i < 2; ++i) {
    auto t = __builtin_amdgcn_permlane16_swap(asu(a[i]), asu(a[i + 2]), false, false);
    const unsigned t0 = t[0], t1 = t[1];
    float s = asf(t0) + asf(t1);
    s = dpp_add<0x128>(s); s = dpp_add<0x124>(s); s = dpp_add<0x122>(s); s = dpp_add<0x121>(s);
    r[i] = s;
  }
}
__device__ __forceinline__ float bcast(const float (&r)[2], int row) {
  return asf((unsigned)__builtin_amdgcn_readlane((int)asu(r[row & 1]), (row >> 1) * 16));
}

#define STAMP_ASM(n) ""
#define STAMP(i) do {} while (0)

#define DMA_GROUP(V0, V1, V2, V3, SRD, SO, STEP, END, START) \
  "s_add_u32 m0, %[ring], slot*4096\n" \
  "s_nop 0\n" \
  "buffer_load_dwordx4 " V0 ", " SRD ", " SO " offen lds\n" \
  "buffer_load_dwordx4 " V1 ", " SRD ", " SO " offen offset:1024 lds\n" \
  "buffer_load_dwordx4 " V2 ", " SRD ", " SO " offen offset:2048 lds\n" \
  "buffer_load_dwordx4 " V3 ", " SRD ", " SO " offen offset:3072 lds\n" \
  "s_add_u32 " SO ", " SO ", " STEP "\n" \
  "s_cmp_eq_u32 " SO ", " END "\n" \
  "s_cselect_b32 " SO ", " START ", " SO "\n"
#define DMA_W1 DMA_GROUP("%[vw0]", "%[vw1]", "%[vw2]", "%[vw3]", "%[srdw]", "%[sow]", "32*2752", "%[sowe]", "%[sows]")
#define DMA_W2 DMA_GROUP("%[vx0]", "%[vx1]", "%[vx2]", "%[vx3]", "%[srdx]", "%[sox]", "32*4096", "%[soxe]", "%[soxs]")
#define TR_GROUP(TOFF) \
  "ds_read_b64_tr_b16 a[16*g+0:16*g+1], %[tr0] offset:(g%%6)*4096\n" \
  "ds_read_b64_tr_b16 a[16*g+2:16*g+3], %[tr0] offset:(g%%6)*4096+" TOFF "\n" \
  "ds_read_b64_tr_b16 a[16*g+4:16*g+5], %[tr1] offset:(g%%6)*4096\n" \
  "ds_read_b64_tr_b16 a[16*g+6:16*g+7], %[tr1] offset:(g%%6)*4096+" TOFF "\n" \
  "ds_read_b64_tr_b16 a[16*g+8:16*g+9], %[tr0] offset:(g%%6)*4096+2048\n" \
  "ds_read_b64_tr_b16 a[16*g+10:16*g+11], %[tr0] offset:(g%%6)*4096+2048+" TOFF "\n" \
  "ds_read_b64_tr_b16 a[16*g+12:16*g+13], %[tr1] offset:(g%%6)*4096+2048\n" \
  "ds_read_b64_tr_b16 a[16*g+14:16*g+15], %[tr1] offset:(g%%6)*4096+2048+" TOFF "\n"
#define ACT_LOAD(ST) \
  "buffer_load_dwordx4 v[128+16*sb+8*" ST ":131+16*sb+8*" ST "], %[va], %[srda], %[soa] offen offset:" ST "*2048 sc1\n" \
  "buffer_load_dwordx4 v[132+16*sb+8*" ST ":135+16*sb+8*" ST "], %[vb], %[srda], %[soa] offen offset:" ST "*2048 sc1\n"
#define ACT_STEP \
  "s_add_u32 %[soa], %[soa], 4096\n" \
  "s_cmp_eq_u32 %[soa], %[soae]\n" \
  "s_cselect_b32 %[soa], %[soas], %[soa]\n"
#define POLL(L) \
  "s_mov_b32 %[cnt], 0x100000\n" \
  L "0%=:\n" \
  "buffer_load_dword v192, %[vfl], %[srdc], 0 offen sc1\n" \
  "s_waitcnt vmcnt(0)\n" \
  "v_cmp_ne_u32 vcc, %[tag], v192\n" \
  "s_and_b64 vcc, vcc, %[pmask]\n" \
  "s_cbranch_scc0 " L "1%=\n" \
  "s_sub_u32 %[cnt], %[cnt], 1\n" \
  "s_cbranch_scc0 " L "0%=\n" \
  L "1%=:\n"
#define XCHG \
  "s_nop 7\n s_nop 7\n" \
  ".set r, 0\n .rept 4\n ds_write_b128 %[xw1], v[80+4*r:83+4*r] offset:r*1024\n .set r, r+1\n .endr\n" \
  ".set r, 0\n .rept 4\n ds_write_b128 %[xw2], v[96+4*r:99+4*r] offset:4096+r*1024\n .set r, r+1\n .endr\n" \
  ".set r, 0\n .rept 4\n ds_write_b128 %[xw3], v[112+4*r:115+4*r] offset:8192+r*1024\n .set r, r+1\n .endr\n" \
  "s_waitcnt lgkmcnt(0)\n" \
  "s_barrier\n" \
  ".set r, 0\n .rept 12\n ds_read_b128 v[80+4*r:83+4*r], %[xr] offset:r*1024\n .set r, r+1\n .endr\n" \
  "s_waitcnt lgkmcnt(8)\n" \
  ".set r, 0\n .rept 8\n v_pk_add_f32 v[64+2*r:65+2*r], v[64+2*r:65+2*r], v[80+2*r:81+2*r]\n .set r, r+1\n .endr\n" \
  "s_waitcnt lgkmcnt(4)\n" \
  ".set r, 0\n .rept 8\n v_pk_add_f32 v[64+2*r:65+2*r], v[64+2*r:65+2*r], v[96+2*r:97+2*r]\n .set r, r+1\n .endr\n" \
  "s_waitcnt lgkmcnt(0)\n" \
  ".set r, 0\n .rept 8\n v_pk_add_f32 v[64+2*r:65+2*r], v[64+2*r:65+2*r], v[112+2*r:113+2*r]\n .set r, r+1\n .endr\n"

__global__ __launch_bounds__(256, 1) void mixer_kernel(const unsigned short* __restrict__ x, const unsigned short* __restrict__ w1,
                                                       const unsigned short* __restrict__ b1, const unsigned short* __restrict__ w2,
                                                       const unsigned short* __restrict__ b2, unsigned short* __restrict__ out,
                                                       unsigned char* __restrict__ ws) {
  extern __shared__ __attribute__((aligned(16))) unsigned char lds[];
  STAMP(0);
  const unsigned long long amp0 = __builtin_amdgcn_s_memrealtime();
  const int lane = threadIdx.x & 63;
  const int w = __builtin_amdgcn_readfirstlane(threadIdx.x >> 6);
  const int b = blockIdx.x, xc = b & 7, j = b >> 3, half = xc & 1, q = xc >> 1;
  const unsigned ldsb = (unsigned)(uintptr_t)(LAS unsigned char*)lds;
  const i32x4 srdc = make_srd(ws, WS_CTRL);
  const __amdgpu_buffer_rsrc_t rsc = __builtin_amdgcn_make_buffer_rsrc(ws, 0, 8 << 20, 0x00020000);

  const int hh = lane >> 5, blk = (lane >> 4) & 1, tq = (lane & 15) >> 2, tp = lane & 3;
  const unsigned ring = ldsb + L_RING + w * RING_W;
  const unsigned trc = ring + 32 * blk + 8 * tp;
  const unsigned sw0 = 64 * ((w >> 1) ^ (tq >> 1)), sw1 = sw0 ^ 64;
  const unsigned va = lane * 16 + (w & 1) * 1024, vb = va ^ 1024;
  const unsigned chunk = ((lane & 7) ^ (((lane >> 4) & 1) << 2)) * 16;
  const unsigned xw1 = ldsb + L_XCH + (w ^ 1) * 12288 + lane * 16, xw2 = ldsb + L_XCH + (w ^ 2) * 12288 + lane * 16,
                 xw3 = ldsb + L_XCH + (w ^ 3) * 12288 + lane * 16, xr = ldsb + L_XCH + w * 12288 + lane * 16;
  const int st = q * 8 + (j & 7), dt2 = half * 4 + (j >> 3);
  const i32x4 srdx = make_srd(w2 + st * 64, (unsigned)(F * P2 - st * 128));
  const unsigned vx0 = (lane >> 3) * P2 + chunk, vx1 = vx0 + 8 * P2 - 1024, vx2 = vx0 + 16 * P2 - 2048, vx3 = vx0 + 24 * P2 - 3072;
  const int rot2 = ((j >> 3) & 1) ^ (j & 1) ? 5 : 0;
  const unsigned soxs = (unsigned)w * (352 * P2), soxe = soxs + 352 * P2;
  unsigned sox = soxs + rot2 * (32 * P2);
  unsigned tag;

  if (j < NHELP) {
    if (j < 8) {
      const int u = (xc * 8 + j) * 4 + w;
      u32x4 xv[8];
#pragma unroll
      for (int i = 0; i < 8; ++i) xv[i] = *(const GAS u32x4*)((const GAS unsigned char*)x + (size_t)(8 * u + i) * 1024 + lane * 16);
      const unsigned e = __builtin_amdgcn_raw_buffer_load_b32(rsc, WS_E, 0, 16);
      float s8[8];
      const bf16x2 ones = {(__bf16)1.0f, (__bf16)1.0f};
#pragma unroll
      for (int i = 0; i < 8; ++i) {
        float s = 0.f;
#pragma unroll
        for (int c = 0; c < 4; ++c) { const unsigned t = xv[i][c]; s = __builtin_amdgcn_fdot2_f32_bf16(__builtin_bit_cast(bf16x2, t), ones, s, false); }
        s8[i] = s;
      }
      STAMP(1);
      float rm[2];
      reduce8(s8, rm);
      rm[0] *= (1.0f / D); rm[1] *= (1.0f / D);
      f32x2 dv[4][8];
      float q8[8];
#pragma unroll
      for (int p = 0; p < 4; ++p) {
        const f32x2 m = {bcast(rm, 2 * p), bcast(rm, 2 * p + 1)};
        f32x2 qq = {0.f, 0.f};
#pragma unroll
        for (int c = 0; c < 8; ++c) {
          const unsigned t0 = xv[2 * p][c >> 1], t1 = xv[2 * p + 1][c >> 1];
          f32x2 v = (c & 1) ? f32x2{asf(t0 & 0xffff0000u), asf(t1 & 0xffff0000u)} : f32x2{asf(t0 << 16), asf(t1 << 16)};
          v -= m;
          dv[p][c] = v;
          qq += v * v;
        }
        q8[2 * p] = qq[0]; q8[2 * p + 1] = qq[1];
      }
      float rv[2];
      reduce8(q8, rv);
      rv[0] = __builtin_amdgcn_rsqf(rv[0] * (1.0f / D) + 1e-5f);
      rv[1] = __builtin_amdgcn_rsqf(rv[1] * (1.0f / D) + 1e-5f);
      f32x2 rs[4];
#pragma unroll
      for (int p = 0; p < 4; ++p) rs[p] = f32x2{bcast(rv, 2 * p), bcast(rv, 2 * p + 1)};
      const unsigned ho = WS_H + (lane >> 3) * H_TILE + (u >> 1) * 2048 + (u & 1) * 512 + (lane & 7) * 16;
#pragma unroll
      for (int c = 0; c < 8; ++c) {
        u32x4 o;
#pragma unroll
        for (int p = 0; p < 4; ++p) { const f32x2 v = dv[p][c] * rs[p]; o[p] = pk_bf16(v[0], v[1]); }
        __builtin_amdgcn_raw_buffer_store_b128(o, rsc, ho + (c >> 2) * 1024 + (c & 3) * 128, 0, 16);
      }
      STAMP(2);
      tag = __builtin_amdgcn_readfirstlane(e) * 0x9E3779B1u + 0x7F4A7C15u;
      asm volatile("s_waitcnt vmcnt(0)" ::: "memory");
      if (lane == 0) __builtin_amdgcn_raw_buffer_store_b32(tag, rsc, WS_HF + u * 4, 0, 16);
      STAMP(3);
    } else {
      const unsigned e = __builtin_amdgcn_raw_buffer_load_b32(rsc, WS_E, 0, 16);
      tag = __builtin_amdgcn_readfirstlane(e) * 0x9E3779B1u + 0x7F4A7C15u;
    }
    if (lane == 0) __builtin_amdgcn_raw_buffer_store_b32(tag, rsc, WS_R + (b * 4 + w) * 4, 0, 16);
    if (b == 9 * 8) {
      for (unsigned it = 0; it < SPIN_CAP; ++it) {
        const u32x4 v = __builtin_amdgcn_raw_buffer_load_b128(rsc, WS_R + (w * 64 + lane) * 16, 0, 16);
        if (__all(v[0] == tag && v[1] == tag && v[2] == tag && v[3] == tag)) break;
        __builtin_amdgcn_s_sleep(4);
      }
      __builtin_amdgcn_s_barrier();
      if (threadIdx.x == 0) __builtin_amdgcn_raw_buffer_store_b32(tag, rsc, WS_E, 0, 16);
    }
    asm volatile(
        ".set slot, 0\n .rept 6\n" DMA_W2 ".set slot, slot+1\n .endr\n"
        : [sox] "+s"(sox)
        : [ring] "s"(ring), [vx0] "v"(vx0), [vx1] "v"(vx1), [vx2] "v"(vx2), [vx3] "v"(vx3), [srdx] "s"(srdx), [soxe] "s"(soxe), [soxs] "s"(soxs)
        : "memory", "scc");
  } else {
    const int idx = q * 22 + (j - NHELP), ft = idx >> 2, dt = half * 4 + (idx & 3);
    const int f0 = ft < 21 ? ft * 64 : F - 64;
    const i32x4 srdw = make_srd(w1 + f0, (unsigned)(S * P1 - f0 * 2));
    const i32x4 srda = make_srd(ws + WS_H + dt * H_TILE, H_TILE);
    const i32x4 srdb = make_srd(b1 + f0, 128);
    const unsigned vw0 = (lane >> 3) * P1 + chunk, vw1 = vw0 + 8 * P1 - 1024, vw2 = vw0 + 16 * P1 - 2048, vw3 = vw0 + 24 * P1 - 3072;
    const int rot = (((idx & 3) + ft) & 3) * 4 + half * 2;
    const unsigned sows = (unsigned)w * (512 * P1), sowe = sows + 512 * P1;
    unsigned sow = sows + rot * (32 * P1);
    const unsigned soas = (unsigned)w * 65536u, soae = soas + 65536u;
    unsigned soa = soas + rot * 4096;
    const unsigned tr0 = trc + (8 * hh + tq) * 128 + sw0, tr1 = trc + (8 * hh + tq) * 128 + sw1;
    const unsigned vfl = WS_HF + (w * 64 + lane) * 4;
    const unsigned vbias = (w >> 1) * 64 + hh * 8;
    const unsigned vr = WS_R + (b * 4 + w) * 4;
    const unsigned long long pmask = ~0ull;
    unsigned cnt;
    f32x16 acc;
    asm volatile(
        "buffer_load_dword v193, off, %[srdc], 0 sc1\n"
        "buffer_load_dwordx2 v[194:195], %[vbias], %[srdb], 0 offen\n"
        "buffer_load_dwordx2 v[196:197], %[vbias], %[srdb], 0 offen offset:16\n"
        "buffer_load_dwordx2 v[198:199], %[vbias], %[srdb], 0 offen offset:32\n"
        "buffer_load_dwordx2 v[200:201], %[vbias], %[srdb], 0 offen offset:48\n"
        ".set slot, 0\n .rept 5\n" DMA_W1 ".set slot, slot+1\n .endr\n"
        "s_waitcnt vmcnt(20)\n"
        "v_readfirstlane_b32 %[tag], v193\n"
        ".set r, 0\n .rept 8\n v_lshlrev_b32 v[64+2*r], 16, v[194+r]\n v_and_b32 v[65+2*r], 0xffff0000, v[194+r]\n .set r, r+1\n .endr\n"
        "s_mul_i32 %[tag], %[tag], 0x9E3779B1\n"
        "s_add_u32 %[tag], %[tag], 0x7F4A7C15\n"
        "v_mov_b32 v193, %[tag]\n"
        "buffer_store_dword v193, %[vr], %[srdc], 0 offen sc1\n"
        ".set g, 0\n .rept 11\n"
        "s_waitcnt vmcnt(16)\n"
        TR_GROUP("512")
        "s_waitcnt lgkmcnt(8)\n"
        ".set slot, (g+5)%%6\n"
        DMA_W1
        ".set g, g+1\n .endr\n"
        STAMP_ASM(1)
        POLL(".Lp")
        STAMP_ASM(2)
        ".set sb, 0\n .rept 4\n" ACT_LOAD("0") ACT_LOAD("1") ACT_STEP ".set sb, sb+1\n .endr\n"
        ".rept 5\n"
        TR_GROUP("512")
        "s_waitcnt lgkmcnt(8)\n"
        ".set g, g+1\n .endr\n"
        "s_waitcnt lgkmcnt(0)\n"
        ".set g, 0\n .rept 16\n"
        ".set sb, g%%4\n"
        "s_waitcnt vmcnt(14)\n"
        ".if g == 0\n"
        "v_mfma_f32_32x32x16_bf16 v[64:79], a[16*g:16*g+3], v[128+16*sb:131+16*sb], v[64:79]\n"
        "v_mfma_f32_32x32x16_bf16 v[80:95], a[16*g:16*g+3], v[132+16*sb:135+16*sb], 0\n"
        "v_mfma_f32_32x32x16_bf16 v[96:111], a[16*g+4:16*g+7], v[128+16*sb:131+16*sb], 0\n"
        "v_mfma_f32_32x32x16_bf16 v[112:127], a[16*g+4:16*g+7], v[132+16*sb:135+16*sb], 0\n"
        ".else\n"
        "v_mfma_f32_32x32x16_bf16 v[64:79], a[16*g:16*g+3], v[128+16*sb:131+16*sb], v[64:79]\n"
        "v_mfma_f32_32x32x16_bf16 v[80:95], a[16*g:16*g+3], v[132+16*sb:135+16*sb], v[80:95]\n"
        "v_mfma_f32_32x32x16_bf16 v[96:111], a[16*g+4:16*g+7], v[128+16*sb:131+16*sb], v[96:111]\n"
        "v_mfma_f32_32x32x16_bf16 v[112:127], a[16*g+4:16*g+7], v[132+16*sb:135+16*sb], v[112:127]\n"
        ".endif\n"
        ".if g < 12\n" ACT_LOAD("0") "s_waitcnt vmcnt(14)\n" ".else\n" "s_waitcnt vmcnt(12)\n" ".endif\n"
        "v_mfma_f32_32x32x16_bf16 v[80:95], a[16*g+8:16*g+11], v[140+16*sb:143+16*sb], v[80:95]\n"
        "v_mfma_f32_32x32x16_bf16 v[96:111], a[16*g+12:16*g+15], v[136+16*sb:139+16*sb], v[96:111]\n"
        "v_mfma_f32_32x32x16_bf16 v[112:127], a[16*g+12:16*g+15], v[140+16*sb:143+16*sb], v[112:127]\n"
        "v_mfma_f32_32x32x16_bf16 v[64:79], a[16*g+8:16*g+11], v[136+16*sb:139+16*sb], v[64:79]\n"
        ".if g < 12\n" ACT_LOAD("1") ACT_STEP ".else\n" ".set slot, g-12\n" DMA_W2 ".endif\n"
        ".set g, g+1\n .endr\n"
        ".set slot, 4\n" DMA_W2 ".set slot, 5\n" DMA_W2
        STAMP_ASM(3)
        XCHG
        : "=&{v[64:79]}"(acc), [sow] "+s"(sow), [soa] "+s"(soa), [sox] "+s"(sox), [cnt] "=&s"(cnt), [tag] "=&s"(tag)
        : [ring] "s"(ring), [vw0] "v"(vw0), [vw1] "v"(vw1), [vw2] "v"(vw2), [vw3] "v"(vw3), [srdw] "s"(srdw), [sowe] "s"(sowe), [sows] "s"(sows),
          [vx0] "v"(vx0), [vx1] "v"(vx1), [vx2] "v"(vx2), [vx3] "v"(vx3), [srdx] "s"(srdx), [soxe] "s"(soxe), [soxs] "s"(soxs),
          [tr0] "v"(tr0), [tr1] "v"(tr1), [va] "v"(va), [vb] "v"(vb), [srda] "s"(srda), [soae] "s"(soae), [soas] "s"(soas),
          [vfl] "v"(vfl), [srdc] "s"(srdc), [pmask] "s"(pmask), [vbias] "v"(vbias), [srdb] "s"(srdb), [vr] "v"(vr),
          [xw1] "v"(xw1), [xw2] "v"(xw2), [xw3] "v"(xw3), [xr] "v"(xr)
        : "memory", "scc", "vcc", VCLOB_ALL, ACLOB_ALL);
    STAMP(4);
    u32x4 o[2];
#pragma unroll
    for (int r = 0; r < 8; ++r) {
      const f32x2 u = {acc[2 * r], acc[2 * r + 1]};
      constexpr float k0 = -2.0f * 0.7978845608028654f * 1.4426950408889634f, k1 = k0 * 0.044715f;
      const f32x2 t = u * (u * u * k1 + k0);
      const f32x2 dn = f32x2{__builtin_amdgcn_exp2f(t[0]), __builtin_amdgcn_exp2f(t[1])} + 1.0f;
      const f32x2 g = u * f32x2{__builtin_amdgcn_rcpf(dn[0]), __builtin_amdgcn_rcpf(dn[1])};
      o[r >> 2][r & 3] = pk_bf16(g[0], g[1]);
    }
    const int f32t = (f0 >> 5) + (w >> 1);
    if (ft < 21 || w >= 2) {
      const unsigned go = WS_G + dt * G_TILE + f32t * 4096 + (w & 1) * 1024 + lane * 16;
      __builtin_amdgcn_raw_buffer_store_b128(o[0], rsc, go, 0, 16);
      __builtin_amdgcn_raw_buffer_store_b128(o[1], rsc, go + 2048, 0, 16);
      STAMP(5);
      asm volatile("s_waitcnt vmcnt(0)" ::: "memory");
      if (lane == 0) __builtin_amdgcn_raw_buffer_store_b32(tag, rsc, WS_GF + (dt * 128 + f32t * 2 + (w & 1)) * 4, 0, 16);
    }
  }
  STAMP(6);
  {
    const i32x4 srda = make_srd(ws + WS_G + dt2 * G_TILE, G_TILE);
    const i32x4 srdb = make_srd(b2 + st * 64, 128);
    const unsigned soas = (unsigned)w * (11 * 4096), soae = soas + 11 * 4096;
    unsigned soa = soas + rot2 * 4096;
    const unsigned tr0 = trc + (4 * hh + tq) * 128 + sw0, tr1 = trc + (4 * hh + tq) * 128 + sw1;
    const unsigned vfl = WS_GF + (dt2 * 128 + w * 22 + lane) * 4;
    const unsigned long long pmask = w < 3 ? (1ull << 22) - 1 : (1ull << 20) - 1;
    const unsigned vbias = (w >> 1) * 64 + (lane & 31) * 2;
    unsigned cnt;
    f32x16 acc;
    asm volatile(
        "buffer_load_ushort v194, %[vbias], %[srdb], 0 offen\n"
        ".set g, 0\n .rept 6\n"
        ".if g == 0\n s_waitcnt vmcnt(21)\n .else\n s_waitcnt vmcnt(16)\n .endif\n"
        TR_GROUP("1024")
        ".if g > 0\n"
        "s_waitcnt lgkmcnt(8)\n"
        ".set slot, (g+5)%%6\n"
        DMA_W2
        ".endif\n"
        ".set g, g+1\n .endr\n"
        "s_barrier\n"
        STAMP_ASM(7)
        POLL(".Lq")
        STAMP_ASM(8)
        ".set sb, 0\n .rept 4\n" ACT_LOAD("0") ACT_LOAD("1") ACT_STEP ".set sb, sb+1\n .endr\n"
        "v_lshlrev_b32 v194, 16, v194\n"
        ".rept 5\n"
        TR_GROUP("1024")
        "s_waitcnt lgkmcnt(8)\n"
        ".set g, g+1\n .endr\n"
        ".set r, 0\n .rept 16\n v_mov_b32 v[64+r], v194\n .set r, r+1\n .endr\n"
        "s_waitcnt lgkmcnt(0)\n"
        ".set g, 0\n .rept 11\n"
        ".set sb, g%%4\n"
        ".if g < 8\n s_waitcnt vmcnt(14)\n .else\n s_waitcnt vmcnt(4*(10-g)+2)\n .endif\n"
        ".if g == 0\n"
        "v_mfma_f32_32x32x16_bf16 v[64:79], v[128+16*sb:131+16*sb], a[16*g:16*g+3], v[64:79]\n"
        "v_mfma_f32_32x32x16_bf16 v[80:95], v[132+16*sb:135+16*sb], a[16*g:16*g+3], 0\n"
        "v_mfma_f32_32x32x16_bf16 v[96:111], v[128+16*sb:131+16*sb], a[16*g+4:16*g+7], 0\n"
        "v_mfma_f32_32x32x16_bf16 v[112:127], v[132+16*sb:135+16*sb], a[16*g+4:16*g+7], 0\n"
        ".else\n"
        "v_mfma_f32_32x32x16_bf16 v[64:79], v[128+16*sb:131+16*sb], a[16*g:16*g+3], v[64:79]\n"
        "v_mfma_f32_32x32x16_bf16 v[80:95], v[132+16*sb:135+16*sb], a[16*g:16*g+3], v[80:95]\n"
        "v_mfma_f32_32x32x16_bf16 v[96:111], v[128+16*sb:131+16*sb], a[16*g+4:16*g+7], v[96:111]\n"
        "v_mfma_f32_32x32x16_bf16 v[112:127], v[132+16*sb:135+16*sb], a[16*g+4:16*g+7], v[112:127]\n"
        ".endif\n"
        ".if g < 7\n" ACT_LOAD("0") "s_waitcnt vmcnt(14)\n" ".else\n" "s_waitcnt vmcnt(4*(10-g))\n" ".endif\n"
        "v_mfma_f32_32x32x16_bf16 v[80:95], v[140+16*sb:143+16*sb], a[16*g+8:16*g+11], v[80:95]\n"
        "v_mfma_f32_32x32x16_bf16 v[96:111], v[136+16*sb:139+16*sb], a[16*g+12:16*g+15], v[96:111]\n"
        "v_mfma_f32_32x32x16_bf16 v[112:127], v[140+16*sb:143+16*sb], a[16*g+12:16*g+15], v[112:127]\n"
        "v_mfma_f32_32x32x16_bf16 v[64:79], v[136+16*sb:139+16*sb], a[16*g+8:16*g+11], v[64:79]\n"
        ".if g < 7\n" ACT_LOAD("1") ACT_STEP ".endif\n"
        ".set g, g+1\n .endr\n"
        STAMP_ASM(9)
        XCHG
        : "=&{v[64:79]}"(acc), [soa] "+s"(soa), [sox] "+s"(sox), [cnt] "=&s"(cnt)
        : [ring] "s"(ring), [vx0] "v"(vx0), [vx1] "v"(vx1), [vx2] "v"(vx2), [vx3] "v"(vx3), [srdx] "s"(srdx), [soxe] "s"(soxe), [soxs] "s"(soxs),
          [tr0] "v"(tr0), [tr1] "v"(tr1), [va] "v"(va), [vb] "v"(vb), [srda] "s"(srda), [soae] "s"(soae), [soas] "s"(soas),
          [vfl] "v"(vfl), [srdc] "s"(srdc), [pmask] "s"(pmask), [vbias] "v"(vbias), [srdb] "s"(srdb), [tag] "s"(tag),
          [xw1] "v"(xw1), [xw2] "v"(xw2), [xw3] "v"(xw3), [xr] "v"(xr)
        : "memory", "scc", "vcc", VCLOB_ALL, ACLOB_ALL);
    STAMP(10);
    LAS unsigned char* ot = (LAS unsigned char*)lds + L_OUT;
    {
      const unsigned o = ((w >> 1) * 32 + (lane & 31)) * 144 + (32 * hh + 4 * (w & 1)) * 2;
#pragma unroll
      for (int e = 0; e < 4; ++e)
        *(LAS u32x2*)(ot + o + e * 16) = u32x2{pk_bf16(acc[e], acc[4 + e]), pk_bf16(acc[8 + e], acc[12 + e])};
    }
    __syncthreads();
#pragma unroll
    for (int i = 0; i < 2; ++i) {
      const int row = w * 16 + i * 8 + (lane >> 3);
      const u32x4 v = *(LAS u32x4*)(ot + row * 144 + (lane & 7) * 16);
      *(GAS u32x4*)((GAS unsigned char*)out + (size_t)(st * 64 + row) * 1024 + dt2 * 128 + (lane & 7) * 16) = v;
    }
  }
  { asm volatile("s_waitcnt vmcnt(0)" ::: "memory"); const unsigned long long amp1 = __builtin_amdgcn_s_memrealtime(); unsigned long long* pp = (unsigned long long*)(ws + (32<<20)); const unsigned long long e0 = pp[0]; const unsigned long long until = amp1 + (amp0 - e0) * PREF; if ( amp0 - e0 < 100000) while (__builtin_amdgcn_s_memrealtime() < until) __builtin_amdgcn_s_sleep(2); if (lane == 0) atomicMax(pp + 1, (unsigned long long)__builtin_amdgcn_s_memrealtime()); }
}

__global__ void k_pre(unsigned long long* p) { if (threadIdx.x == 0) { p[1] = 0; p[0] = __builtin_amdgcn_s_memrealtime(); } }
__global__ void k_post(unsigned long long* p) {
  const unsigned long long t = __builtin_amdgcn_s_memrealtime();
  const unsigned long long e = p[1];
  const unsigned long long until = t + (t - e) * POSTF;
  if (t - e < 100000) while (__builtin_amdgcn_s_memrealtime() < until) __builtin_amdgcn_s_sleep(2);
}

extern "C" void kernel_launch(void* const* d_in, const int* in_sizes, int n_in, void* d_out, int out_size, void* d_ws,
                              size_t ws_size, hipStream_t stream) {
  (void)hipFuncSetAttribute((const void*)mixer_kernel, hipFuncAttributeMaxDynamicSharedMemorySize, LDS_BYTES);
  k_pre<<<1,64,0,stream>>>((unsigned long long*)((char*)d_ws + (32<<20)));
  mixer_kernel<<<256, 256, LDS_BYTES, stream>>>((const unsigned short*)d_in[0], (const unsigned short*)d_in[1],
                                                (const unsigned short*)d_in[2], (const unsigned short*)d_in[3],
                                                (const unsigned short*)d_in[4], (unsigned short*)d_out, (unsigned char*)d_ws);
  k_post<<<1,64,0,stream>>>((unsigned long long*)((char*)d_ws + (32<<20)));
}
```

```cpp
#include <hip/hip_runtime.h>
#include <hip/hip_bf16.h>
#include <stdint.h>

#define PROFILE 0

typedef float f32x16 __attribute__((ext_vector_type(16)));
typedef float f32x2 __attribute__((ext_vector_type(2)));
typedef int i32x4 __attribute__((ext_vector_type(4)));
typedef unsigned u32x4 __attribute__((ext_vector_type(4)));
typedef unsigned u32x2 __attribute__((ext_vector_type(2)));
typedef __bf16 bf16x2 __attribute__((ext_vector_type(2)));
#define GAS __attribute__((address_space(1)))
#define LAS __attribute__((address_space(3)))

constexpr int S = 2048, D = 512, F = 1376;
constexpr int P1 = F * 2, P2 = S * 2;
constexpr int WS_E = 0, WS_R = 4096, WS_HF = 8192, WS_GF = 12288, WS_CTRL = 32768;
constexpr int WS_DBG = 65536;
constexpr int WS_H = 1 << 20, H_TILE = 128 * 2048;
constexpr int WS_G = 4 << 20, G_TILE = 86 * 2048;
constexpr int L_XCH = 0, L_OUT = 49152, L_RING = 65536, RING_W = 24576, LDS_BYTES = 163840;
constexpr int NHELP = 10;
constexpr unsigned SPIN_CAP = 1u << 20;

#define ACLOB10(p) "a" #p "0", "a" #p "1", "a" #p "2", "a" #p "3", "a" #p "4", "a" #p "5", "a" #p "6", "a" #p "7", "a" #p "8", "a" #p "9"
#define ACLOB_ALL "a0","a1","a2","a3","a4","a5","a6","a7","a8","a9", ACLOB10(1), ACLOB10(2), ACLOB10(3), ACLOB10(4), ACLOB10(5), ACLOB10(6), ACLOB10(7), ACLOB10(8), ACLOB10(9), \
  ACLOB10(10), ACLOB10(11), ACLOB10(12), ACLOB10(13), ACLOB10(14), ACLOB10(15), ACLOB10(16), ACLOB10(17), ACLOB10(18), ACLOB10(19), ACLOB10(20), ACLOB10(21), ACLOB10(22), ACLOB10(23), ACLOB10(24), \
  "a250","a251","a252","a253","a254","a255"
#define VCLOB10(p) "v" #p "0", "v" #p "1", "v" #p "2", "v" #p "3", "v" #p "4", "v" #p "5", "v" #p "6", "v" #p "7", "v" #p "8", "v" #p "9"
#define VCLOB_ALL VCLOB10(8), VCLOB10(9), VCLOB10(10), VCLOB10(11), VCLOB10(12), VCLOB10(13), VCLOB10(14), VCLOB10(15), VCLOB10(16), VCLOB10(17), VCLOB10(18), VCLOB10(19), VCLOB10(20)

__device__ __forceinline__ i32x4 make_srd(const void* p, unsigned bytes) {
  const uint64_t a = (uint64_t)p;
  return i32x4{(int)(uint32_t)a, (int)(uint32_t)(a >> 32), (int)bytes, 0x00020000};
}
__device__ __forceinline__ float asf(unsigned u) { return __builtin_bit_cast(float, u); }
__device__ __forceinline__ unsigned asu(float f) { return __builtin_bit_cast(unsigned, f); }
__device__ __forceinline__ unsigned pk_bf16(float lo, float hi) {
  bf16x2 v = {(__bf16)lo, (__bf16)hi};
  return __builtin_bit_cast(unsigned, v);
}
template <int CTRL> __device__ __forceinline__ float dpp_add(float v) {
  return v + asf((unsigned)__builtin_amdgcn_update_dpp(0, (int)asu(v), CTRL, 0xf, 0xf, true));
}
__device__ __forceinline__ void reduce8(const float (&v)[8], float (&r)[2]) {
  float a[4];
#pragma unroll
  for (int i = 0; i < 4; ++i) {
    auto t = __builtin_amdgcn_permlane32_swap(asu(v[i]), asu(v[i + 4]), false, false);
    const unsigned t0 = t[0], t1 = t[1];
    a[i] = asf(t0) + asf(t1);
  }
#pragma unroll
  for (int i = 0; i < 2; ++i) {
    auto t = __builtin_amdgcn_permlane16_swap(asu(a[i]), asu(a[i + 2]), false, false);
    const unsigned t0 = t[0], t1 = t[1];
    float s = asf(t0) + asf(t1);
    s = dpp_add<0x128>(s); s = dpp_add<0x124>(s); s = dpp_add<0x122>(s); s = dpp_add<0x121>(s);
    r[i] = s;
  }
}
__device__ __forceinline__ float bcast(const float (&r)[2], int row) {
  return asf((unsigned)__builtin_amdgcn_readlane((int)asu(r[row & 1]), (row >> 1) * 16));
}

#define STAMP_ASM(n) ""
#define STAMP(i) do {} while (0)

#define DMA_GROUP(V0, V1, V2, V3, SRD, SO, STEP, END, START) \
  "s_add_u32 m0, %[ring], slot*4096\n" \
  "s_nop 0\n" \
  "buffer_load_dwordx4 " V0 ", " SRD ", " SO " offen lds\n" \
  "buffer_load_dwordx4 " V1 ", " SRD ", " SO " offen offset:1024 lds\n" \
  "buffer_load_dwordx4 " V2 ", " SRD ", " SO " offen offset:2048 lds\n" \
  "buffer_load_dwordx4 " V3 ", " SRD ", " SO " offen offset:3072 lds\n" \
  "s_add_u32 " SO ", " SO ", " STEP "\n" \
  "s_cmp_eq_u32 " SO ", " END "\n" \
  "s_cselect_b32 " SO ", " START ", " SO "\n"
#define DMA_W1 DMA_GROUP("%[vw0]", "%[vw1]", "%[vw2]", "%[vw3]", "%[srdw]", "%[sow]", "32*2752", "%[sowe]", "%[sows]")
#define DMA_W2 DMA_GROUP("%[vx0]", "%[vx1]", "%[vx2]", "%[vx3]", "%[srdx]", "%[sox]", "32*4096", "%[soxe]", "%[soxs]")
#define TR_GROUP(TOFF) \
  "ds_read_b64_tr_b16 a[16*g+0:16*g+1], %[tr0] offset:(g%%6)*4096\n" \
  "ds_read_b64_tr_b16 a[16*g+2:16*g+3], %[tr0] offset:(g%%6)*4096+" TOFF "\n" \
  "ds_read_b64_tr_b16 a[16*g+4:16*g+5], %[tr1] offset:(g%%6)*4096\n" \
  "ds_read_b64_tr_b16 a[16*g+6:16*g+7], %[tr1] offset:(g%%6)*4096+" TOFF "\n" \
  "ds_read_b64_tr_b16 a[16*g+8:16*g+9], %[tr0] offset:(g%%6)*4096+2048\n" \
  "ds_read_b64_tr_b16 a[16*g+10:16*g+11], %[tr0] offset:(g%%6)*4096+2048+" TOFF "\n" \
  "ds_read_b64_tr_b16 a[16*g+12:16*g+13], %[tr1] offset:(g%%6)*4096+2048\n" \
  "ds_read_b64_tr_b16 a[16*g+14:16*g+15], %[tr1] offset:(g%%6)*4096+2048+" TOFF "\n"
#define ACT_LOAD(ST) \
  "buffer_load_dwordx4 v[128+16*sb+8*" ST ":131+16*sb+8*" ST "], %[va], %[srda], %[soa] offen offset:" ST "*2048 sc1\n" \
  "buffer_load_dwordx4 v[132+16*sb+8*" ST ":135+16*sb+8*" ST "], %[vb], %[srda], %[soa] offen offset:" ST "*2048 sc1\n"
#define ACT_STEP \
  "s_add_u32 %[soa], %[soa], 4096\n" \
  "s_cmp_eq_u32 %[soa], %[soae]\n" \
  "s_cselect_b32 %[soa], %[soas], %[soa]\n"
#define POLL(L) \
  "s_mov_b32 %[cnt], 0x100000\n" \
  L "0%=:\n" \
  "buffer_load_dword v192, %[vfl], %[srdc], 0 offen sc1\n" \
  "s_waitcnt vmcnt(0)\n" \
  "v_cmp_ne_u32 vcc, %[tag], v192\n" \
  "s_and_b64 vcc, vcc, %[pmask]\n" \
  "s_cbranch_scc0 " L "1%=\n" \
  "s_sub_u32 %[cnt], %[cnt], 1\n" \
  "s_cbranch_scc0 " L "0%=\n" \
  L "1%=:\n"
#define XCHG \
  "s_nop 7\n s_nop 7\n" \
  ".set r, 0\n .rept 4\n ds_write_b128 %[xw1], v[80+4*r:83+4*r] offset:r*1024\n .set r, r+1\n .endr\n" \
  ".set r, 0\n .rept 4\n ds_write_b128 %[xw2], v[96+4*r:99+4*r] offset:4096+r*1024\n .set r, r+1\n .endr\n" \
  ".set r, 0\n .rept 4\n ds_write_b128 %[xw3], v[112+4*r:115+4*r] offset:8192+r*1024\n .set r, r+1\n .endr\n" \
  "s_waitcnt lgkmcnt(0)\n" \
  "s_barrier\n" \
  ".set r, 0\n .rept 12\n ds_read_b128 v[80+4*r:83+4*r], %[xr] offset:r*1024\n .set r, r+1\n .endr\n" \
  "s_waitcnt lgkmcnt(8)\n" \
  ".set r, 0\n .rept 8\n v_pk_add_f32 v[64+2*r:65+2*r], v[64+2*r:65+2*r], v[80+2*r:81+2*r]\n .set r, r+1\n .endr\n" \
  "s_waitcnt lgkmcnt(4)\n" \
  ".set r, 0\n .rept 8\n v_pk_add_f32 v[64+2*r:65+2*r], v[64+2*r:65+2*r], v[96+2*r:97+2*r]\n .set r, r+1\n .endr\n" \
  "s_waitcnt lgkmcnt(0)\n" \
  ".set r, 0\n .rept 8\n v_pk_add_f32 v[64+2*r:65+2*r], v[64+2*r:65+2*r], v[112+2*r:113+2*r]\n .set r, r+1\n .endr\n"

__global__ __launch_bounds__(256, 1) void mixer_kernel(const unsigned short* __restrict__ x, const unsigned short* __restrict__ w1,
                                                       const unsigned short* __restrict__ b1, const unsigned short* __restrict__ w2,
                                                       const unsigned short* __restrict__ b2, unsigned short* __restrict__ out,
                                                       unsigned char* __restrict__ ws) {
  extern __shared__ __attribute__((aligned(16))) unsigned char lds[];
  STAMP(0);
  const int lane = threadIdx.x & 63;
  const int w = __builtin_amdgcn_readfirstlane(threadIdx.x >> 6);
  const int b = blockIdx.x, xc = b & 7, j = b >> 3, half = xc & 1, q = xc >> 1;
  const unsigned ldsb = (unsigned)(uintptr_t)(LAS unsigned char*)lds;
  const i32x4 srdc = make_srd(ws, WS_CTRL);
  const __amdgpu_buffer_rsrc_t rsc = __builtin_amdgcn_make_buffer_rsrc(ws, 0, 8 << 20, 0x00020000);

  const int hh = lane >> 5, blk = (lane >> 4) & 1, tq = (lane & 15) >> 2, tp = lane & 3;
  const unsigned ring = ldsb + L_RING + w * RING_W;
  const unsigned trc = ring + 32 * blk + 8 * tp;
  const unsigned sw0 = 64 * ((w >> 1) ^ (tq >> 1)), sw1 = sw0 ^ 64;
  const unsigned va = lane * 16 + (w & 1) * 1024, vb = va ^ 1024;
  const unsigned chunk = ((lane & 7) ^ (((lane >> 4) & 1) << 2)) * 16;
  const unsigned xw1 = ldsb + L_XCH + (w ^ 1) * 12288 + lane * 16, xw2 = ldsb + L_XCH + (w ^ 2) * 12288 + lane * 16,
                 xw3 = ldsb + L_XCH + (w ^ 3) * 12288 + lane * 16, xr = ldsb + L_XCH + w * 12288 + lane * 16;
  const int st = q * 8 + (j & 7), dt2 = half * 4 + (j >> 3);
  const i32x4 srdx = make_srd(w2 + st * 64, (unsigned)(F * P2 - st * 128));
  const unsigned vx0 = (lane >> 3) * P2 + chunk, vx1 = vx0 + 8 * P2 - 1024, vx2 = vx0 + 16 * P2 - 2048, vx3 = vx0 + 24 * P2 - 3072;
  const int rot2 = ((j >> 3) & 1) ^ (j & 1) ? 5 : 0;
  const unsigned soxs = (unsigned)w * (352 * P2), soxe = soxs + 352 * P2;
  unsigned sox = soxs + rot2 * (32 * P2);
  unsigned tag;

  if (j < NHELP) {
    if (j < 8) {
      const int u = (xc * 8 + j) * 4 + w;
      u32x4 xv[8];
#pragma unroll
      for (int i = 0; i < 8; ++i) xv[i] = *(const GAS u32x4*)((const GAS unsigned char*)x + (size_t)(8 * u + i) * 1024 + lane * 16);
      const unsigned e = __builtin_amdgcn_raw_buffer_load_b32(rsc, WS_E, 0, 16);
      float s8[8];
      const bf16x2 ones = {(__bf16)1.0f, (__bf16)1.0f};
#pragma unroll
      for (int i = 0; i < 8; ++i) {
        float s = 0.f;
#pragma unroll
        for (int c = 0; c < 4; ++c) { const unsigned t = xv[i][c]; s = __builtin_amdgcn_fdot2_f32_bf16(__builtin_bit_cast(bf16x2, t), ones, s, false); }
        s8[i] = s;
      }
      STAMP(1);
      float rm[2];
      reduce8(s8, rm);
      rm[0] *= (1.0f / D); rm[1] *= (1.0f / D);
      f32x2 dv[4][8];
      float q8[8];
#pragma unroll
      for (int p = 0; p < 4; ++p) {
        const f32x2 m = {bcast(rm, 2 * p), bcast(rm, 2 * p + 1)};
        f32x2 qq = {0.f, 0.f};
#pragma unroll
        for (int c = 0; c < 8; ++c) {
          const unsigned t0 = xv[2 * p][c >> 1], t1 = xv[2 * p + 1][c >> 1];
          f32x2 v = (c & 1) ? f32x2{asf(t0 & 0xffff0000u), asf(t1 & 0xffff0000u)} : f32x2{asf(t0 << 16), asf(t1 << 16)};
          v -= m;
          dv[p][c] = v;
          qq += v * v;
        }
        q8[2 * p] = qq[0]; q8[2 * p + 1] = qq[1];
      }
      float rv[2];
      reduce8(q8, rv);
      rv[0] = __builtin_amdgcn_rsqf(rv[0] * (1.0f / D) + 1e-5f);
      rv[1] = __builtin_amdgcn_rsqf(rv[1] * (1.0f / D) + 1e-5f);
      f32x2 rs[4];
#pragma unroll
      for (int p = 0; p < 4; ++p) rs[p] = f32x2{bcast(rv, 2 * p), bcast(rv, 2 * p + 1)};
      const unsigned ho = WS_H + (lane >> 3) * H_TILE + (u >> 1) * 2048 + (u & 1) * 512 + (lane & 7) * 16;
#pragma unroll
      for (int c = 0; c < 8; ++c) {
        u32x4 o;
#pragma unroll
        for (int p = 0; p < 4; ++p) { const f32x2 v = dv[p][c] * rs[p]; o[p] = pk_bf16(v[0], v[1]); }
        __builtin_amdgcn_raw_buffer_store_b128(o, rsc, ho + (c >> 2) * 1024 + (c & 3) * 128, 0, 16);
      }
      STAMP(2);
      tag = __builtin_amdgcn_readfirstlane(e) * 0x9E3779B1u + 0x7F4A7C15u;
      asm volatile("s_waitcnt vmcnt(0)" ::: "memory");
      if (lane == 0) __builtin_amdgcn_raw_buffer_store_b32(tag, rsc, WS_HF + u * 4, 0, 16);
      STAMP(3);
    } else {
      const unsigned e = __builtin_amdgcn_raw_buffer_load_b32(rsc, WS_E, 0, 16);
      tag = __builtin_amdgcn_readfirstlane(e) * 0x9E3779B1u + 0x7F4A7C15u;
    }
    if (lane == 0) __builtin_amdgcn_raw_buffer_store_b32(tag, rsc, WS_R + (b * 4 + w) * 4, 0, 16);
    if (b == 9 * 8) {
      for (unsigned it = 0; it < SPIN_CAP; ++it) {
        const u32x4 v = __builtin_amdgcn_raw_buffer_load_b128(rsc, WS_R + (w * 64 + lane) * 16, 0, 16);
        if (__all(v[0] == tag && v[1] == tag && v[2] == tag && v[3] == tag)) break;
        __builtin_amdgcn_s_sleep(4);
      }
      __builtin_amdgcn_s_barrier();
      if (threadIdx.x == 0) __builtin_amdgcn_raw_buffer_store_b32(tag, rsc, WS_E, 0, 16);
    }
    asm volatile(
        ".set slot, 0\n .rept 6\n" DMA_W2 ".set slot, slot+1\n .endr\n"
        : [sox] "+s"(sox)
        : [ring] "s"(ring), [vx0] "v"(vx0), [vx1] "v"(vx1), [vx2] "v"(vx2), [vx3] "v"(vx3), [srdx] "s"(srdx), [soxe] "s"(soxe), [soxs] "s"(soxs)
        : "memory", "scc");
  } else {
    const int idx = q * 22 + (j - NHELP), ft = idx >> 2, dt = half * 4 + (idx & 3);
    const int f0 = ft < 21 ? ft * 64 : F - 64;
    const i32x4 srdw = make_srd(w1 + f0, (unsigned)(S * P1 - f0 * 2));
    const i32x4 srda = make_srd(ws + WS_H + dt * H_TILE, H_TILE);
    const i32x4 srdb = make_srd(b1 + f0, 128);
    const unsigned vw0 = (lane >> 3) * P1 + chunk, vw1 = vw0 + 8 * P1 - 1024, vw2 = vw0 + 16 * P1 - 2048, vw3 = vw0 + 24 * P1 - 3072;
    const int rot = (((idx & 3) + ft) & 3) * 4 + half * 2;
    const unsigned sows = (unsigned)w * (512 * P1), sowe = sows + 512 * P1;
    unsigned sow = sows + rot * (32 * P1);
    const unsigned soas = (unsigned)w * 65536u, soae = soas + 65536u;
    unsigned soa = soas + rot * 4096;
    const unsigned tr0 = trc + (8 * hh + tq) * 128 + sw0, tr1 = trc + (8 * hh + tq) * 128 + sw1;
    const unsigned vfl = WS_HF + (w * 64 + lane) * 4;
    const unsigned vbias = (w >> 1) * 64 + hh * 8;
    const unsigned vr = WS_R + (b * 4 + w) * 4;
    const unsigned long long pmask = ~0ull;
    unsigned cnt;
    f32x16 acc;
    asm volatile(
        "buffer_load_dword v193, off, %[srdc], 0 sc1\n"
        "buffer_load_dwordx2 v[194:195], %[vbias], %[srdb], 0 offen\n"
        "buffer_load_dwordx2 v[196:197], %[vbias], %[srdb], 0 offen offset:16\n"
        "buffer_load_dwordx2 v[198:199], %[vbias], %[srdb], 0 offen offset:32\n"
        "buffer_load_dwordx2 v[200:201], %[vbias], %[srdb], 0 offen offset:48\n"
        ".set slot, 0\n .rept 5\n" DMA_W1 ".set slot, slot+1\n .endr\n"
        "s_waitcnt vmcnt(20)\n"
        "v_readfirstlane_b32 %[tag], v193\n"
        ".set r, 0\n .rept 8\n v_lshlrev_b32 v[64+2*r], 16, v[194+r]\n v_and_b32 v[65+2*r], 0xffff0000, v[194+r]\n .set r, r+1\n .endr\n"
        "s_mul_i32 %[tag], %[tag], 0x9E3779B1\n"
        "s_add_u32 %[tag], %[tag], 0x7F4A7C15\n"
        "v_mov_b32 v193, %[tag]\n"
        "buffer_store_dword v193, %[vr], %[srdc], 0 offen sc1\n"
        ".set g, 0\n .rept 11\n"
        "s_waitcnt vmcnt(16)\n"
        TR_GROUP("512")
        "s_waitcnt lgkmcnt(8)\n"
        ".set slot, (g+5)%%6\n"
        DMA_W1
        ".set g, g+1\n .endr\n"
        STAMP_ASM(1)
        POLL(".Lp")
        STAMP_ASM(2)
        ".set sb, 0\n .rept 4\n" ACT_LOAD("0") ACT_LOAD("1") ACT_STEP ".set sb, sb+1\n .endr\n"
        ".rept 5\n"
        TR_GROUP("512")
        "s_waitcnt lgkmcnt(8)\n"
        ".set g, g+1\n .endr\n"
        "s_waitcnt lgkmcnt(0)\n"
        ".set g, 0\n .rept 16\n"
        ".set sb, g%%4\n"
        "s_waitcnt vmcnt(14)\n"
        ".if g == 0\n"
        "v_mfma_f32_32x32x16_bf16 v[64:79], a[16*g:16*g+3], v[128+16*sb:131+16*sb], v[64:79]\n"
        "v_mfma_f32_32x32x16_bf16 v[80:95], a[16*g:16*g+3], v[132+16*sb:135+16*sb], 0\n"
        "v_mfma_f32_32x32x16_bf16 v[96:111], a[16*g+4:16*g+7], v[128+16*sb:131+16*sb], 0\n"
        "v_mfma_f32_32x32x16_bf16 v[112:127], a[16*g+4:16*g+7], v[132+16*sb:135+16*sb], 0\n"
        ".else\n"
        "v_mfma_f32_32x32x16_bf16 v[64:79], a[16*g:16*g+3], v[128+16*sb:131+16*sb], v[64:79]\n"
        "v_mfma_f32_32x32x16_bf16 v[80:95], a[16*g:16*g+3], v[132+16*sb:135+16*sb], v[80:95]\n"
        "v_mfma_f32_32x32x16_bf16 v[96:111], a[16*g+4:16*g+7], v[128+16*sb:131+16*sb], v[96:111]\n"
        "v_mfma_f32_32x32x16_bf16 v[112:127], a[16*g+4:16*g+7], v[132+16*sb:135+16*sb], v[112:127]\n"
        ".endif\n"
        ".if g < 12\n" ACT_LOAD("0") "s_waitcnt vmcnt(14)\n" ".else\n" "s_waitcnt vmcnt(12)\n" ".endif\n"
        "v_mfma_f32_32x32x16_bf16 v[80:95], a[16*g+8:16*g+11], v[140+16*sb:143+16*sb], v[80:95]\n"
        "v_mfma_f32_32x32x16_bf16 v[96:111], a[16*g+12:16*g+15], v[136+16*sb:139+16*sb], v[96:111]\n"
        "v_mfma_f32_32x32x16_bf16 v[112:127], a[16*g+12:16*g+15], v[140+16*sb:143+16*sb], v[112:127]\n"
        "v_mfma_f32_32x32x16_bf16 v[64:79], a[16*g+8:16*g+11], v[136+16*sb:139+16*sb], v[64:79]\n"
        ".if g < 12\n" ACT_LOAD("1") ACT_STEP ".else\n" ".set slot, g-12\n" DMA_W2 ".endif\n"
        ".set g, g+1\n .endr\n"
        ".set slot, 4\n" DMA_W2 ".set slot, 5\n" DMA_W2
        STAMP_ASM(3)
        XCHG
        : "=&{v[64:79]}"(acc), [sow] "+s"(sow), [soa] "+s"(soa), [sox] "+s"(sox), [cnt] "=&s"(cnt), [tag] "=&s"(tag)
        : [ring] "s"(ring), [vw0] "v"(vw0), [vw1] "v"(vw1), [vw2] "v"(vw2), [vw3] "v"(vw3), [srdw] "s"(srdw), [sowe] "s"(sowe), [sows] "s"(sows),
          [vx0] "v"(vx0), [vx1] "v"(vx1), [vx2] "v"(vx2), [vx3] "v"(vx3), [srdx] "s"(srdx), [soxe] "s"(soxe), [soxs] "s"(soxs),
          [tr0] "v"(tr0), [tr1] "v"(tr1), [va] "v"(va), [vb] "v"(vb), [srda] "s"(srda), [soae] "s"(soae), [soas] "s"(soas),
          [vfl] "v"(vfl), [srdc] "s"(srdc), [pmask] "s"(pmask), [vbias] "v"(vbias), [srdb] "s"(srdb), [vr] "v"(vr),
          [xw1] "v"(xw1), [xw2] "v"(xw2), [xw3] "v"(xw3), [xr] "v"(xr)
        : "memory", "scc", "vcc", VCLOB_ALL, ACLOB_ALL);
    STAMP(4);
    u32x4 o[2];
#pragma unroll
    for (int r = 0; r < 8; ++r) {
      const f32x2 u = {acc[2 * r], acc[2 * r + 1]};
      constexpr float k0 = -2.0f * 0.7978845608028654f * 1.4426950408889634f, k1 = k0 * 0.044715f;
      const f32x2 t = u * (u * u * k1 + k0);
      const f32x2 dn = f32x2{__builtin_amdgcn_exp2f(t[0]), __builtin_amdgcn_exp2f(t[1])} + 1.0f;
      const f32x2 g = u * f32x2{__builtin_amdgcn_rcpf(dn[0]), __builtin_amdgcn_rcpf(dn[1])};
      o[r >> 2][r & 3] = pk_bf16(g[0], g[1]);
    }
    const int f32t = (f0 >> 5) + (w >> 1);
    if (ft < 21 || w >= 2) {
      const unsigned go = WS_G + dt * G_TILE + f32t * 4096 + (w & 1) * 1024 + lane * 16;
      __builtin_amdgcn_raw_buffer_store_b128(o[0], rsc, go, 0, 16);
      __builtin_amdgcn_raw_buffer_store_b128(o[1], rsc, go + 2048, 0, 16);
      STAMP(5);
      asm volatile("s_waitcnt vmcnt(0)" ::: "memory");
      if (lane == 0) __builtin_amdgcn_raw_buffer_store_b32(tag, rsc, WS_GF + (dt * 128 + f32t * 2 + (w & 1)) * 4, 0, 16);
    }
  }
  STAMP(6);
  {
    const i32x4 srda = make_srd(ws + WS_G + dt2 * G_TILE, G_TILE);
    const i32x4 srdb = make_srd(b2 + st * 64, 128);
    const unsigned soas = (unsigned)w * (11 * 4096), soae = soas + 11 * 4096;
    unsigned soa = soas + rot2 * 4096;
    const unsigned tr0 = trc + (4 * hh + tq) * 128 + sw0, tr1 = trc + (4 * hh + tq) * 128 + sw1;
    const unsigned vfl = WS_GF + (dt2 * 128 + w * 22 + lane) * 4;
    const unsigned long long pmask = w < 3 ? (1ull << 22) - 1 : (1ull << 20) - 1;
    const unsigned vbias = (w >> 1) * 64 + (lane & 31) * 2;
    unsigned cnt;
    f32x16 acc;
    asm volatile(
        "buffer_load_ushort v194, %[vbias], %[srdb], 0 offen\n"
        ".set g, 0\n .rept 6\n"
        ".if g == 0\n s_waitcnt vmcnt(21)\n .else\n s_waitcnt vmcnt(16)\n .endif\n"
        TR_GROUP("1024")
        ".if g > 0\n"
        "s_waitcnt lgkmcnt(8)\n"
        ".set slot, (g+5)%%6\n"
        DMA_W2
        ".endif\n"
        ".set g, g+1\n .endr\n"
        "s_barrier\n"
        STAMP_ASM(7)
        POLL(".Lq")
        STAMP_ASM(8)
        ".set sb, 0\n .rept 4\n" ACT_LOAD("0") ACT_LOAD("1") ACT_STEP ".set sb, sb+1\n .endr\n"
        "v_lshlrev_b32 v194, 16, v194\n"
        ".rept 5\n"
        TR_GROUP("1024")
        "s_waitcnt lgkmcnt(8)\n"
        ".set g, g+1\n .endr\n"
        ".set r, 0\n .rept 16\n v_mov_b32 v[64+r], v194\n .set r, r+1\n .endr\n"
        "s_waitcnt lgkmcnt(0)\n"
        ".set g, 0\n .rept 11\n"
        ".set sb, g%%4\n"
        ".if g < 8\n s_waitcnt vmcnt(14)\n .else\n s_waitcnt vmcnt(4*(10-g)+2)\n .endif\n"
        ".if g == 0\n"
        "v_mfma_f32_32x32x16_bf16 v[64:79], v[128+16*sb:131+16*sb], a[16*g:16*g+3], v[64:79]\n"
        "v_mfma_f32_32x32x16_bf16 v[80:95], v[132+16*sb:135+16*sb], a[16*g:16*g+3], 0\n"
        "v_mfma_f32_32x32x16_bf16 v[96:111], v[128+16*sb:131+16*sb], a[16*g+4:16*g+7], 0\n"
        "v_mfma_f32_32x32x16_bf16 v[112:127], v[132+16*sb:135+16*sb], a[16*g+4:16*g+7], 0\n"
        ".else\n"
        "v_mfma_f32_32x32x16_bf16 v[64:79], v[128+16*sb:131+16*sb], a[16*g:16*g+3], v[64:79]\n"
        "v_mfma_f32_32x32x16_bf16 v[80:95], v[132+16*sb:135+16*sb], a[16*g:16*g+3], v[80:95]\n"
        "v_mfma_f32_32x32x16_bf16 v[96:111], v[128+16*sb:131+16*sb], a[16*g+4:16*g+7], v[96:111]\n"
        "v_mfma_f32_32x32x16_bf16 v[112:127], v[132+16*sb:135+16*sb], a[16*g+4:16*g+7], v[112:127]\n"
        ".endif\n"
        ".if g < 7\n" ACT_LOAD("0") "s_waitcnt vmcnt(14)\n" ".else\n" "s_waitcnt vmcnt(4*(10-g))\n" ".endif\n"
        "v_mfma_f32_32x32x16_bf16 v[80:95], v[140+16*sb:143+16*sb], a[16*g+8:16*g+11], v[80:95]\n"
        "v_mfma_f32_32x32x16_bf16 v[96:111], v[136+16*sb:139+16*sb], a[16*g+12:16*g+15], v[96:111]\n"
        "v_mfma_f32_32x32x16_bf16 v[112:127], v[140+16*sb:143+16*sb], a[16*g+12:16*g+15], v[112:127]\n"
        "v_mfma_f32_32x32x16_bf16 v[64:79], v[136+16*sb:139+16*sb], a[16*g+8:16*g+11], v[64:79]\n"
        ".if g < 7\n" ACT_LOAD("1") ACT_STEP ".endif\n"
        ".set g, g+1\n .endr\n"
        STAMP_ASM(9)
        XCHG
        : "=&{v[64:79]}"(acc), [soa] "+s"(soa), [sox] "+s"(sox), [cnt] "=&s"(cnt)
        : [ring] "s"(ring), [vx0] "v"(vx0), [vx1] "v"(vx1), [vx2] "v"(vx2), [vx3] "v"(vx3), [srdx] "s"(srdx), [soxe] "s"(soxe), [soxs] "s"(soxs),
          [tr0] "v"(tr0), [tr1] "v"(tr1), [va] "v"(va), [vb] "v"(vb), [srda] "s"(srda), [soae] "s"(soae), [soas] "s"(soas),
          [vfl] "v"(vfl), [srdc] "s"(srdc), [pmask] "s"(pmask), [vbias] "v"(vbias), [srdb] "s"(srdb), [tag] "s"(tag),
          [xw1] "v"(xw1), [xw2] "v"(xw2), [xw3] "v"(xw3), [xr] "v"(xr)
        : "memory", "scc", "vcc", VCLOB_ALL, ACLOB_ALL);
    STAMP(10);
    LAS unsigned char* ot = (LAS unsigned char*)lds + L_OUT;
    {
      const unsigned o = ((w >> 1) * 32 + (lane & 31)) * 144 + (32 * hh + 4 * (w & 1)) * 2;
#pragma unroll
      for (int e = 0; e < 4; ++e)
        *(LAS u32x2*)(ot + o + e * 16) = u32x2{pk_bf16(acc[e], acc[4 + e]), pk_bf16(acc[8 + e], acc[12 + e])};
    }
    __syncthreads();
#pragma unroll
    for (int i = 0; i < 2; ++i) {
      const int row = w * 16 + i * 8 + (lane >> 3);
      const u32x4 v = *(LAS u32x4*)(ot + row * 144 + (lane & 7) * 16);
      *(GAS u32x4*)((GAS unsigned char*)out + (size_t)(st * 64 + row) * 1024 + dt2 * 128 + (lane & 7) * 16) = v;
    }
  }
}

__global__ __launch_bounds__(256,1) void k_big(unsigned* p) { const unsigned long long t = __builtin_amdgcn_s_memrealtime(); while (__builtin_amdgcn_s_memrealtime() < t + 1000) __builtin_amdgcn_s_sleep(2); }
extern "C" void kernel_launch(void* const* d_in, const int* in_sizes, int n_in, void* d_out, int out_size, void* d_ws,
                              size_t ws_size, hipStream_t stream) {
  (void)hipFuncSetAttribute((const void*)mixer_kernel, hipFuncAttributeMaxDynamicSharedMemorySize, LDS_BYTES);
  mixer_kernel<<<256, 256, LDS_BYTES, stream>>>((const unsigned short*)d_in[0], (const unsigned short*)d_in[1],
                                                (const unsigned short*)d_in[2], (const unsigned short*)d_in[3],
                                                (const unsigned short*)d_in[4], (unsigned short*)d_out, (unsigned char*)d_ws);
  (void)hipFuncSetAttribute((const void*)k_big, hipFuncAttributeMaxDynamicSharedMemorySize, LDS_BYTES);
  k_big<<<256,256,LDS_BYTES,stream>>>((unsigned*)d_ws);
}
```

```cpp
#define POSTF 0
#define PREF 0
#define AMPF 0
#include <hip/hip_runtime.h>
#include <hip/hip_bf16.h>
#include <stdint.h>

#define PROFILE 0

typedef float f32x16 __attribute__((ext_vector_type(16)));
typedef float f32x2 __attribute__((ext_vector_type(2)));
typedef int i32x4 __attribute__((ext_vector_type(4)));
typedef unsigned u32x4 __attribute__((ext_vector_type(4)));
typedef unsigned u32x2 __attribute__((ext_vector_type(2)));
typedef __bf16 bf16x2 __attribute__((ext_vector_type(2)));
#define GAS __attribute__((address_space(1)))
#define LAS __attribute__((address_space(3)))

constexpr int S = 2048, D = 512, F = 1376;
constexpr int P1 = F * 2, P2 = S * 2;
constexpr int WS_E = 0, WS_R = 4096, WS_HF = 8192, WS_GF = 12288, WS_CTRL = 32768;
constexpr int WS_DBG = 65536;
constexpr int WS_H = 1 << 20, H_TILE = 128 * 2048;
constexpr int WS_G = 4 << 20, G_TILE = 86 * 2048;
constexpr int L_XCH = 0, L_OUT = 49152, L_RING = 65536, RING_W = 24576, LDS_BYTES = 163840;
constexpr int NHELP = 10;
constexpr unsigned SPIN_CAP = 1u << 20;

#define ACLOB10(p) "a" #p "0", "a" #p "1", "a" #p "2", "a" #p "3", "a" #p "4", "a" #p "5", "a" #p "6", "a" #p "7", "a" #p "8", "a" #p "9"
#define ACLOB_ALL "a0","a1","a2","a3","a4","a5","a6","a7","a8","a9", ACLOB10(1), ACLOB10(2), ACLOB10(3), ACLOB10(4), ACLOB10(5), ACLOB10(6), ACLOB10(7), ACLOB10(8), ACLOB10(9), \
  ACLOB10(10), ACLOB10(11), ACLOB10(12), ACLOB10(13), ACLOB10(14), ACLOB10(15), ACLOB10(16), ACLOB10(17), ACLOB10(18), ACLOB10(19), ACLOB10(20), ACLOB10(21), ACLOB10(22), ACLOB10(23), ACLOB10(24), \
  "a250","a251","a252","a253","a254","a255"
#define VCLOB10(p) "v" #p "0", "v" #p "1", "v" #p "2", "v" #p "3", "v" #p "4", "v" #p "5", "v" #p "6", "v" #p "7", "v" #p "8", "v" #p "9"
#define VCLOB_ALL VCLOB10(8), VCLOB10(9), VCLOB10(10), VCLOB10(11), VCLOB10(12), VCLOB10(13), VCLOB10(14), VCLOB10(15), VCLOB10(16), VCLOB10(17), VCLOB10(18), VCLOB10(19), VCLOB10(20)

__device__ __forceinline__ i32x4 make_srd(const void* p, unsigned bytes) {
  const uint64_t a = (uint64_t)p;
  return i32x4{(int)(uint32_t)a, (int)(uint32_t)(a >> 32), (int)bytes, 0x00020000};
}
__device__ __forceinline__ float asf(unsigned u) { return __builtin_bit_cast(float, u); }
__device__ __forceinline__ unsigned asu(float f) { return __builtin_bit_cast(unsigned, f); }
__device__ __forceinline__ unsigned pk_bf16(float lo, float hi) {
  bf16x2 v = {(__bf16)lo, (__bf16)hi};
  return __builtin_bit_cast(unsigned, v);
}
template <int CTRL> __device__ __forceinline__ float dpp_add(float v) {
  return v + asf((unsigned)__builtin_amdgcn_update_dpp(0, (int)asu(v), CTRL, 0xf, 0xf, true));
}
__device__ __forceinline__ void reduce8(const float (&v)[8], float (&r)[2]) {
  float a[4];
#pragma unroll
  for (int i = 0; i < 4; ++i) {
    auto t = __builtin_amdgcn_permlane32_swap(asu(v[i]), asu(v[i + 4]), false, false);
    const unsigned t0 = t[0], t1 = t[1];
    a[i] = asf(t0) + asf(t1);
  }
#pragma unroll
  for (int i = 0; i < 2; ++i) {
    auto t = __builtin_amdgcn_permlane16_swap(asu(a[i]), asu(a[i + 2]), false, false);
    const unsigned t0 = t[0], t1 = t[1];
    float s = asf(t0) + asf(t1);
    s = dpp_add<0x128>(s); s = dpp_add<0x124>(s); s = dpp_add<0x122>(s); s = dpp_add<0x121>(s);
    r[i] = s;
  }
}
__device__ __forceinline__ float bcast(const float (&r)[2], int row) {
  return asf((unsigned)__builtin_amdgcn_readlane((int)asu(r[row & 1]), (row >> 1) * 16));
}

#define STAMP_ASM(n) ""
#define STAMP(i) do {} while (0)

#define DMA_GROUP(V0, V1, V2, V3, SRD, SO, STEP, END, START) \
  "s_add_u32 m0, %[ring], slot*4096\n" \
  "s_nop 0\n" \
  "buffer_load_dwordx4 " V0 ", " SRD ", " SO " offen lds\n" \
  "buffer_load_dwordx4 " V1 ", " SRD ", " SO " offen offset:1024 lds\n" \
  "buffer_load_dwordx4 " V2 ", " SRD ", " SO " offen offset:2048 lds\n" \
  "buffer_load_dwordx4 " V3 ", " SRD ", " SO " offen offset:3072 lds\n" \
  "s_add_u32 " SO ", " SO ", " STEP "\n" \
  "s_cmp_eq_u32 " SO ", " END "\n" \
  "s_cselect_b32 " SO ", " START ", " SO "\n"
#define DMA_W1 DMA_GROUP("%[vw0]", "%[vw1]", "%[vw2]", "%[vw3]", "%[srdw]", "%[sow]", "32*2752", "%[sowe]", "%[sows]")
#define DMA_W2 DMA_GROUP("%[vx0]", "%[vx1]", "%[vx2]", "%[vx3]", "%[srdx]", "%[sox]", "32*4096", "%[soxe]", "%[soxs]")
#define TR_GROUP(TOFF) \
  "ds_read_b64_tr_b16 a[16*g+0:16*g+1], %[tr0] offset:(g%%6)*4096\n" \
  "ds_read_b64_tr_b16 a[16*g+2:16*g+3], %[tr0] offset:(g%%6)*4096+" TOFF "\n" \
  "ds_read_b64_tr_b16 a[16*g+4:16*g+5], %[tr1] offset:(g%%6)*4096\n" \
  "ds_read_b64_tr_b16 a[16*g+6:16*g+7], %[tr1] offset:(g%%6)*4096+" TOFF "\n" \
  "ds_read_b64_tr_b16 a[16*g+8:16*g+9], %[tr0] offset:(g%%6)*4096+2048\n" \
  "ds_read_b64_tr_b16 a[16*g+10:16*g+11], %[tr0] offset:(g%%6)*4096+2048+" TOFF "\n" \
  "ds_read_b64_tr_b16 a[16*g+12:16*g+13], %[tr1] offset:(g%%6)*4096+2048\n" \
  "ds_read_b64_tr_b16 a[16*g+14:16*g+15], %[tr1] offset:(g%%6)*4096+2048+" TOFF "\n"
#define ACT_LOAD(ST) \
  "buffer_load_dwordx4 v[128+16*sb+8*" ST ":131+16*sb+8*" ST "], %[va], %[srda], %[soa] offen offset:" ST "*2048 sc1\n" \
  "buffer_load_dwordx4 v[132+16*sb+8*" ST ":135+16*sb+8*" ST "], %[vb], %[srda], %[soa] offen offset:" ST "*2048 sc1\n"
#define ACT_STEP \
  "s_add_u32 %[soa], %[soa], 4096\n" \
  "s_cmp_eq_u32 %[soa], %[soae]\n" \
  "s_cselect_b32 %[soa], %[soas], %[soa]\n"
#define POLL(L) \
  "s_mov_b32 %[cnt], 0x100000\n" \
  L "0%=:\n" \
  "buffer_load_dword v192, %[vfl], %[srdc], 0 offen sc1\n" \
  "s_waitcnt vmcnt(0)\n" \
  "v_cmp_ne_u32 vcc, %[tag], v192\n" \
  "s_and_b64 vcc, vcc, %[pmask]\n" \
  "s_cbranch_scc0 " L "1%=\n" \
  "s_sub_u32 %[cnt], %[cnt], 1\n" \
  "s_cbranch_scc0 " L "0%=\n" \
  L "1%=:\n"
#define XCHG \
  "s_nop 7\n s_nop 7\n" \
  ".set r, 0\n .rept 4\n ds_write_b128 %[xw1], v[80+4*r:83+4*r] offset:r*1024\n .set r, r+1\n .endr\n" \
  ".set r, 0\n .rept 4\n ds_write_b128 %[xw2], v[96+4*r:99+4*r] offset:4096+r*1024\n .set r, r+1\n .endr\n" \
  ".set r, 0\n .rept 4\n ds_write_b128 %[xw3], v[112+4*r:115+4*r] offset:8192+r*1024\n .set r, r+1\n .endr\n" \
  "s_waitcnt lgkmcnt(0)\n" \
  "s_barrier\n" \
  ".set r, 0\n .rept 12\n ds_read_b128 v[80+4*r:83+4*r], %[xr] offset:r*1024\n .set r, r+1\n .endr\n" \
  "s_waitcnt lgkmcnt(8)\n" \
  ".set r, 0\n .rept 8\n v_pk_add_f32 v[64+2*r:65+2*r], v[64+2*r:65+2*r], v[80+2*r:81+2*r]\n .set r, r+1\n .endr\n" \
  "s_waitcnt lgkmcnt(4)\n" \
  ".set r, 0\n .rept 8\n v_pk_add_f32 v[64+2*r:65+2*r], v[64+2*r:65+2*r], v[96+2*r:97+2*r]\n .set r, r+1\n .endr\n" \
  "s_waitcnt lgkmcnt(0)\n" \
  ".set r, 0\n .rept 8\n v_pk_add_f32 v[64+2*r:65+2*r], v[64+2*r:65+2*r], v[112+2*r:113+2*r]\n .set r, r+1\n .endr\n"

__global__ __launch_bounds__(256, 1) void mixer_kernel(const unsigned short* __restrict__ x, const unsigned short* __restrict__ w1,
                                                       const unsigned short* __restrict__ b1, const unsigned short* __restrict__ w2,
                                                       const unsigned short* __restrict__ b2, unsigned short* __restrict__ out,
                                                       unsigned char* __restrict__ ws) {
  extern __shared__ __attribute__((aligned(16))) unsigned char lds[];
  STAMP(0);
  const unsigned long long amp0 = __builtin_amdgcn_s_memrealtime();
  const int lane = threadIdx.x & 63;
  const int w = __builtin_amdgcn_readfirstlane(threadIdx.x >> 6);
  const int b = blockIdx.x, xc = b & 7, j = b >> 3, half = xc & 1, q = xc >> 1;
  const unsigned ldsb = (unsigned)(uintptr_t)(LAS unsigned char*)lds;
  const i32x4 srdc = make_srd(ws, WS_CTRL);
  const __amdgpu_buffer_rsrc_t rsc = __builtin_amdgcn_make_buffer_rsrc(ws, 0, 8 << 20, 0x00020000);

  const int hh = lane >> 5, blk = (lane >> 4) & 1, tq = (lane & 15) >> 2, tp = lane & 3;
  const unsigned ring = ldsb + L_RING + w * RING_W;
  const unsigned trc = ring + 32 * blk + 8 * tp;
  const unsigned sw0 = 64 * ((w >> 1) ^ (tq >> 1)), sw1 = sw0 ^ 64;
  const unsigned va = lane * 16 + (w & 1) * 1024, vb = va ^ 1024;
  const unsigned chunk = ((lane & 7) ^ (((lane >> 4) & 1) << 2)) * 16;
  const unsigned xw1 = ldsb + L_XCH + (w ^ 1) * 12288 + lane * 16, xw2 = ldsb + L_XCH + (w ^ 2) * 12288 + lane * 16,
                 xw3 = ldsb + L_XCH + (w ^ 3) * 12288 + lane * 16, xr = ldsb + L_XCH + w * 12288 + lane * 16;
  const int st = q * 8 + (j & 7), dt2 = half * 4 + (j >> 3);
  const i32x4 srdx = make_srd(w2 + st * 64, (unsigned)(F * P2 - st * 128));
  const unsigned vx0 = (lane >> 3) * P2 + chunk, vx1 = vx0 + 8 * P2 - 1024, vx2 = vx0 + 16 * P2 - 2048, vx3 = vx0 + 24 * P2 - 3072;
  const int rot2 = ((j >> 3) & 1) ^ (j & 1) ? 5 : 0;
  const unsigned soxs = (unsigned)w * (352 * P2), soxe = soxs + 352 * P2;
  unsigned sox = soxs + rot2 * (32 * P2);
  unsigned tag;

  if (j < NHELP) {
    if (j < 8) {
      const int u = (xc * 8 + j) * 4 + w;
      u32x4 xv[8];
#pragma unroll
      for (int i = 0; i < 8; ++i) xv[i] = *(const GAS u32x4*)((const GAS unsigned char*)x + (size_t)(8 * u + i) * 1024 + lane * 16);
      const unsigned e = __builtin_amdgcn_raw_buffer_load_b32(rsc, WS_E, 0, 16);
      float s8[8];
      const bf16x2 ones = {(__bf16)1.0f, (__bf16)1.0f};
#pragma unroll
      for (int i = 0; i < 8; ++i) {
        float s = 0.f;
#pragma unroll
        for (int c = 0; c < 4; ++c) { const unsigned t = xv[i][c]; s = __builtin_amdgcn_fdot2_f32_bf16(__builtin_bit_cast(bf16x2, t), ones, s, false); }
        s8[i] = s;
      }
      STAMP(1);
      float rm[2];
      reduce8(s8, rm);
      rm[0] *= (1.0f / D); rm[1] *= (1.0f / D);
      f32x2 dv[4][8];
      float q8[8];
#pragma unroll
      for (int p = 0; p < 4; ++p) {
        const f32x2 m = {bcast(rm, 2 * p), bcast(rm, 2 * p + 1)};
        f32x2 qq = {0.f, 0.f};
#pragma unroll
        for (int c = 0; c < 8; ++c) {
          const unsigned t0 = xv[2 * p][c >> 1], t1 = xv[2 * p + 1][c >> 1];
          f32x2 v = (c & 1) ? f32x2{asf(t0 & 0xffff0000u), asf(t1 & 0xffff0000u)} : f32x2{asf(t0 << 16), asf(t1 << 16)};
          v -= m;
          dv[p][c] = v;
          qq += v * v;
        }
        q8[2 * p] = qq[0]; q8[2 * p + 1] = qq[1];
      }
      float rv[2];
      reduce8(q8, rv);
      rv[0] = __builtin_amdgcn_rsqf(rv[0] * (1.0f / D) + 1e-5f);
      rv[1] = __builtin_amdgcn_rsqf(rv[1] * (1.0f / D) + 1e-5f);
      f32x2 rs[4];
#pragma unroll
      for (int p = 0; p < 4; ++p) rs[p] = f32x2{bcast(rv, 2 * p), bcast(rv, 2 * p + 1)};
      const unsigned ho = WS_H + (lane >> 3) * H_TILE + (u >> 1) * 2048 + (u & 1) * 512 + (lane & 7) * 16;
#pragma unroll
      for (int c = 0; c < 8; ++c) {
        u32x4 o;
#pragma unroll
        for (int p = 0; p < 4; ++p) { const f32x2 v = dv[p][c] * rs[p]; o[p] = pk_bf16(v[0], v[1]); }
        __builtin_amdgcn_raw_buffer_store_b128(o, rsc, ho + (c >> 2) * 1024 + (c & 3) * 128, 0, 16);
      }
      STAMP(2);
      tag = __builtin_amdgcn_readfirstlane(e) * 0x9E3779B1u + 0x7F4A7C15u;
      asm volatile("s_waitcnt vmcnt(0)" ::: "memory");
      if (lane == 0) __builtin_amdgcn_raw_buffer_store_b32(tag, rsc, WS_HF + u * 4, 0, 16);
      STAMP(3);
    } else {
      const unsigned e = __builtin_amdgcn_raw_buffer_load_b32(rsc, WS_E, 0, 16);
      tag = __builtin_amdgcn_readfirstlane(e) * 0x9E3779B1u + 0x7F4A7C15u;
    }
    if (lane == 0) __builtin_amdgcn_raw_buffer_store_b32(tag, rsc, WS_R + (b * 4 + w) * 4, 0, 16);
    if (b == 9 * 8) {
      for (unsigned it = 0; it < SPIN_CAP; ++it) {
        const u32x4 v = __builtin_amdgcn_raw_buffer_load_b128(rsc, WS_R + (w * 64 + lane) * 16, 0, 16);
        if (__all(v[0] == tag && v[1] == tag && v[2] == tag && v[3] == tag)) break;
        __builtin_amdgcn_s_sleep(4);
      }
      __builtin_amdgcn_s_barrier();
      if (threadIdx.x == 0) __builtin_amdgcn_raw_buffer_store_b32(tag, rsc, WS_E, 0, 16);
    }
    asm volatile(
        ".set slot, 0\n .rept 6\n" DMA_W2 ".set slot, slot+1\n .endr\n"
        : [sox] "+s"(sox)
        : [ring] "s"(ring), [vx0] "v"(vx0), [vx1] "v"(vx1), [vx2] "v"(vx2), [vx3] "v"(vx3), [srdx] "s"(srdx), [soxe] "s"(soxe), [soxs] "s"(soxs)
        : "memory", "scc");
  } else {
    const int idx = q * 22 + (j - NHELP), ft = idx >> 2, dt = half * 4 + (idx & 3);
    const int f0 = ft < 21 ? ft * 64 : F - 64;
    const i32x4 srdw = make_srd(w1 + f0, (unsigned)(S * P1 - f0 * 2));
    const i32x4 srda = make_srd(ws + WS_H + dt * H_TILE, H_TILE);
    const i32x4 srdb = make_srd(b1 + f0, 128);
    const unsigned vw0 = (lane >> 3) * P1 + chunk, vw1 = vw0 + 8 * P1 - 1024, vw2 = vw0 + 16 * P1 - 2048, vw3 = vw0 + 24 * P1 - 3072;
    const int rot = (((idx & 3) + ft) & 3) * 4 + half * 2;
    const unsigned sows = (unsigned)w * (512 * P1), sowe = sows + 512 * P1;
    unsigned sow = sows + rot * (32 * P1);
    const unsigned soas = (unsigned)w * 65536u, soae = soas + 65536u;
    unsigned soa = soas + rot * 4096;
    const unsigned tr0 = trc + (8 * hh + tq) * 128 + sw0, tr1 = trc + (8 * hh + tq) * 128 + sw1;
    const unsigned vfl = WS_HF + (w * 64 + lane) * 4;
    const unsigned vbias = (w >> 1) * 64 + hh * 8;
    const unsigned vr = WS_R + (b * 4 + w) * 4;
    const unsigned long long pmask = ~0ull;
    unsigned cnt;
    f32x16 acc;
    asm volatile(
        "buffer_load_dword v193, off, %[srdc], 0 sc1\n"
        "buffer_load_dwordx2 v[194:195], %[vbias], %[srdb], 0 offen\n"
        "buffer_load_dwordx2 v[196:197], %[vbias], %[srdb], 0 offen offset:16\n"
        "buffer_load_dwordx2 v[198:199], %[vbias], %[srdb], 0 offen offset:32\n"
        "buffer_load_dwordx2 v[200:201], %[vbias], %[srdb], 0 offen offset:48\n"
        ".set slot, 0\n .rept 5\n" DMA_W1 ".set slot, slot+1\n .endr\n"
        "s_waitcnt vmcnt(20)\n"
        "v_readfirstlane_b32 %[tag], v193\n"
        ".set r, 0\n .rept 8\n v_lshlrev_b32 v[64+2*r], 16, v[194+r]\n v_and_b32 v[65+2*r], 0xffff0000, v[194+r]\n .set r, r+1\n .endr\n"
        "s_mul_i32 %[tag], %[tag], 0x9E3779B1\n"
        "s_add_u32 %[tag], %[tag], 0x7F4A7C15\n"
        "v_mov_b32 v193, %[tag]\n"
        "buffer_store_dword v193, %[vr], %[srdc], 0 offen sc1\n"
        ".set g, 0\n .rept 11\n"
        "s_waitcnt vmcnt(16)\n"
        TR_GROUP("512")
        "s_waitcnt lgkmcnt(8)\n"
        ".set slot, (g+5)%%6\n"
        DMA_W1
        ".set g, g+1\n .endr\n"
        STAMP_ASM(1)
        POLL(".Lp")
        STAMP_ASM(2)
        ".set sb, 0\n .rept 4\n" ACT_LOAD("0") ACT_LOAD("1") ACT_STEP ".set sb, sb+1\n .endr\n"
        ".rept 5\n"
        TR_GROUP("512")
        "s_waitcnt lgkmcnt(8)\n"
        ".set g, g+1\n .endr\n"
        "s_waitcnt lgkmcnt(0)\n"
        ".set g, 0\n .rept 16\n"
        ".set sb, g%%4\n"
        "s_waitcnt vmcnt(14)\n"
        ".if g == 0\n"
        "v_mfma_f32_32x32x16_bf16 v[64:79], a[16*g:16*g+3], v[128+16*sb:131+16*sb], v[64:79]\n"
        "v_mfma_f32_32x32x16_bf16 v[80:95], a[16*g:16*g+3], v[132+16*sb:135+16*sb], 0\n"
        "v_mfma_f32_32x32x16_bf16 v[96:111], a[16*g+4:16*g+7], v[128+16*sb:131+16*sb], 0\n"
        "v_mfma_f32_32x32x16_bf16 v[112:127], a[16*g+4:16*g+7], v[132+16*sb:135+16*sb], 0\n"
        ".else\n"
        "v_mfma_f32_32x32x16_bf16 v[64:79], a[16*g:16*g+3], v[128+16*sb:131+16*sb], v[64:79]\n"
        "v_mfma_f32_32x32x16_bf16 v[80:95], a[16*g:16*g+3], v[132+16*sb:135+16*sb], v[80:95]\n"
        "v_mfma_f32_32x32x16_bf16 v[96:111], a[16*g+4:16*g+7], v[128+16*sb:131+16*sb], v[96:111]\n"
        "v_mfma_f32_32x32x16_bf16 v[112:127], a[16*g+4:16*g+7], v[132+16*sb:135+16*sb], v[112:127]\n"
        ".endif\n"
        ".if g < 12\n" ACT_LOAD("0") "s_waitcnt vmcnt(14)\n" ".else\n" "s_waitcnt vmcnt(12)\n" ".endif\n"
        "v_mfma_f32_32x32x16_bf16 v[80:95], a[16*g+8:16*g+11], v[140+16*sb:143+16*sb], v[80:95]\n"
        "v_mfma_f32_32x32x16_bf16 v[96:111], a[16*g+12:16*g+15], v[136+16*sb:139+16*sb], v[96:111]\n"
        "v_mfma_f32_32x32x16_bf16 v[112:127], a[16*g+12:16*g+15], v[140+16*sb:143+16*sb], v[112:127]\n"
        "v_mfma_f32_32x32x16_bf16 v[64:79], a[16*g+8:16*g+11], v[136+16*sb:139+16*sb], v[64:79]\n"
        ".if g < 12\n" ACT_LOAD("1") ACT_STEP ".else\n" ".set slot, g-12\n" DMA_W2 ".endif\n"
        ".set g, g+1\n .endr\n"
        ".set slot, 4\n" DMA_W2 ".set slot, 5\n" DMA_W2
        STAMP_ASM(3)
        XCHG
        : "=&{v[64:79]}"(acc), [sow] "+s"(sow), [soa] "+s"(soa), [sox] "+s"(sox), [cnt] "=&s"(cnt), [tag] "=&s"(tag)
        : [ring] "s"(ring), [vw0] "v"(vw0), [vw1] "v"(vw1), [vw2] "v"(vw2), [vw3] "v"(vw3), [srdw] "s"(srdw), [sowe] "s"(sowe), [sows] "s"(sows),
          [vx0] "v"(vx0), [vx1] "v"(vx1), [vx2] "v"(vx2), [vx3] "v"(vx3), [srdx] "s"(srdx), [soxe] "s"(soxe), [soxs] "s"(soxs),
          [tr0] "v"(tr0), [tr1] "v"(tr1), [va] "v"(va), [vb] "v"(vb), [srda] "s"(srda), [soae] "s"(soae), [soas] "s"(soas),
          [vfl] "v"(vfl), [srdc] "s"(srdc), [pmask] "s"(pmask), [vbias] "v"(vbias), [srdb] "s"(srdb), [vr] "v"(vr),
          [xw1] "v"(xw1), [xw2] "v"(xw2), [xw3] "v"(xw3), [xr] "v"(xr)
        : "memory", "scc", "vcc", VCLOB_ALL, ACLOB_ALL);
    STAMP(4);
    u32x4 o[2];
#pragma unroll
    for (int r = 0; r < 8; ++r) {
      const f32x2 u = {acc[2 * r], acc[2 * r + 1]};
      constexpr float k0 = -2.0f * 0.7978845608028654f * 1.4426950408889634f, k1 = k0 * 0.044715f;
      const f32x2 t = u * (u * u * k1 + k0);
      const f32x2 dn = f32x2{__builtin_amdgcn_exp2f(t[0]), __builtin_amdgcn_exp2f(t[1])} + 1.0f;
      const f32x2 g = u * f32x2{__builtin_amdgcn_rcpf(dn[0]), __builtin_amdgcn_rcpf(dn[1])};
      o[r >> 2][r & 3] = pk_bf16(g[0], g[1]);
    }
    const int f32t = (f0 >> 5) + (w >> 1);
    if (ft < 21 || w >= 2) {
      const unsigned go = WS_G + dt * G_TILE + f32t * 4096 + (w & 1) * 1024 + lane * 16;
      __builtin_amdgcn_raw_buffer_store_b128(o[0], rsc, go, 0, 16);
      __builtin_amdgcn_raw_buffer_store_b128(o[1], rsc, go + 2048, 0, 16);
      STAMP(5);
      asm volatile("s_waitcnt vmcnt(0)" ::: "memory");
      if (lane == 0) __builtin_amdgcn_raw_buffer_store_b32(tag, rsc, WS_GF + (dt * 128 + f32t * 2 + (w & 1)) * 4, 0, 16);
    }
  }
  STAMP(6);
  {
    const i32x4 srda = make_srd(ws + WS_G + dt2 * G_TILE, G_TILE);
    const i32x4 srdb = make_srd(b2 + st * 64, 128);
    const unsigned soas = (unsigned)w * (11 * 4096), soae = soas + 11 * 4096;
    unsigned soa = soas + rot2 * 4096;
    const unsigned tr0 = trc + (4 * hh + tq) * 128 + sw0, tr1 = trc + (4 * hh + tq) * 128 + sw1;
    const unsigned vfl = WS_GF + (dt2 * 128 + w * 22 + lane) * 4;
    const unsigned long long pmask = w < 3 ? (1ull << 22) - 1 : (1ull << 20) - 1;
    const unsigned vbias = (w >> 1) * 64 + (lane & 31) * 2;
    unsigned cnt;
    f32x16 acc;
    asm volatile(
        "buffer_load_ushort v194, %[vbias], %[srdb], 0 offen\n"
        ".set g, 0\n .rept 6\n"
        ".if g == 0\n s_waitcnt vmcnt(21)\n .else\n s_waitcnt vmcnt(16)\n .endif\n"
        TR_GROUP("1024")
        ".if g > 0\n"
        "s_waitcnt lgkmcnt(8)\n"
        ".set slot, (g+5)%%6\n"
        DMA_W2
        ".endif\n"
        ".set g, g+1\n .endr\n"
        "s_barrier\n"
        STAMP_ASM(7)
        POLL(".Lq")
        STAMP_ASM(8)
        ".set sb, 0\n .rept 4\n" ACT_LOAD("0") ACT_LOAD("1") ACT_STEP ".set sb, sb+1\n .endr\n"
        "v_lshlrev_b32 v194, 16, v194\n"
        ".rept 5\n"
        TR_GROUP("1024")
        "s_waitcnt lgkmcnt(8)\n"
        ".set g, g+1\n .endr\n"
        ".set r, 0\n .rept 16\n v_mov_b32 v[64+r], v194\n .set r, r+1\n .endr\n"
        "s_waitcnt lgkmcnt(0)\n"
        ".set g, 0\n .rept 11\n"
        ".set sb, g%%4\n"
        ".if g < 8\n s_waitcnt vmcnt(14)\n .else\n s_waitcnt vmcnt(4*(10-g)+2)\n .endif\n"
        ".if g == 0\n"
        "v_mfma_f32_32x32x16_bf16 v[64:79], v[128+16*sb:131+16*sb], a[16*g:16*g+3], v[64:79]\n"
        "v_mfma_f32_32x32x16_bf16 v[80:95], v[132+16*sb:135+16*sb], a[16*g:16*g+3], 0\n"
        "v_mfma_f32_32x32x16_bf16 v[96:111], v[128+16*sb:131+16*sb], a[16*g+4:16*g+7], 0\n"
        "v_mfma_f32_32x32x16_bf16 v[112:127], v[132+16*sb:135+16*sb], a[16*g+4:16*g+7], 0\n"
        ".else\n"
        "v_mfma_f32_32x32x16_bf16 v[64:79], v[128+16*sb:131+16*sb], a[16*g:16*g+3], v[64:79]\n"
        "v_mfma_f32_32x32x16_bf16 v[80:95], v[132+16*sb:135+16*sb], a[16*g:16*g+3], v[80:95]\n"
        "v_mfma_f32_32x32x16_bf16 v[96:111], v[128+16*sb:131+16*sb], a[16*g+4:16*g+7], v[96:111]\n"
        "v_mfma_f32_32x32x16_bf16 v[112:127], v[132+16*sb:135+16*sb], a[16*g+4:16*g+7], v[112:127]\n"
        ".endif\n"
        ".if g < 7\n" ACT_LOAD("0") "s_waitcnt vmcnt(14)\n" ".else\n" "s_waitcnt vmcnt(4*(10-g))\n" ".endif\n"
        "v_mfma_f32_32x32x16_bf16 v[80:95], v[140+16*sb:143+16*sb], a[16*g+8:16*g+11], v[80:95]\n"
        "v_mfma_f32_32x32x16_bf16 v[96:111], v[136+16*sb:139+16*sb], a[16*g+12:16*g+15], v[96:111]\n"
        "v_mfma_f32_32x32x16_bf16 v[112:127], v[140+16*sb:143+16*sb], a[16*g+12:16*g+15], v[112:127]\n"
        "v_mfma_f32_32x32x16_bf16 v[64:79], v[136+16*sb:139+16*sb], a[16*g+8:16*g+11], v[64:79]\n"
        ".if g < 7\n" ACT_LOAD("1") ACT_STEP ".endif\n"
        ".set g, g+1\n .endr\n"
        STAMP_ASM(9)
        XCHG
        : "=&{v[64:79]}"(acc), [soa] "+s"(soa), [sox] "+s"(sox), [cnt] "=&s"(cnt)
        : [ring] "s"(ring), [vx0] "v"(vx0), [vx1] "v"(vx1), [vx2] "v"(vx2), [vx3] "v"(vx3), [srdx] "s"(srdx), [soxe] "s"(soxe), [soxs] "s"(soxs),
          [tr0] "v"(tr0), [tr1] "v"(tr1), [va] "v"(va), [vb] "v"(vb), [srda] "s"(srda), [soae] "s"(soae), [soas] "s"(soas),
          [vfl] "v"(vfl), [srdc] "s"(srdc), [pmask] "s"(pmask), [vbias] "v"(vbias), [srdb] "s"(srdb), [tag] "s"(tag),
          [xw1] "v"(xw1), [xw2] "v"(xw2), [xw3] "v"(xw3), [xr] "v"(xr)
        : "memory", "scc", "vcc", VCLOB_ALL, ACLOB_ALL);
    STAMP(10);
    LAS unsigned char* ot = (LAS unsigned char*)lds + L_OUT;
    {
      const unsigned o = ((w >> 1) * 32 + (lane & 31)) * 144 + (32 * hh + 4 * (w & 1)) * 2;
#pragma unroll
      for (int e = 0; e < 4; ++e)
        *(LAS u32x2*)(ot + o + e * 16) = u32x2{pk_bf16(acc[e], acc[4 + e]), pk_bf16(acc[8 + e], acc[12 + e])};
    }
    __syncthreads();
#pragma unroll
    for (int i = 0; i < 2; ++i) {
      const int row = w * 16 + i * 8 + (lane >> 3);
      const u32x4 v = *(LAS u32x4*)(ot + row * 144 + (lane & 7) * 16);
      *(GAS u32x4*)((GAS unsigned char*)out + (size_t)(st * 64 + row) * 1024 + dt2 * 128 + (lane & 7) * 16) = v;
    }
  }
  { asm volatile("s_waitcnt vmcnt(0)" ::: "memory"); const unsigned long long amp1 = __builtin_amdgcn_s_memrealtime(); unsigned long long* pp = (unsigned long long*)(ws + (32<<20)); const unsigned long long e0 = pp[0]; const unsigned long long until = amp1 + (amp0 - e0) * PREF; if ( amp0 - e0 < 100000) while (__builtin_amdgcn_s_memrealtime() < until) __builtin_amdgcn_s_sleep(2); if (lane == 0) pp[64 + (b * 4 + w) * 16] = __builtin_amdgcn_s_memrealtime(); }
}

__global__ void k_pre(unsigned long long* p) { if (threadIdx.x == 0) { p[0] = __builtin_amdgcn_s_memrealtime(); } }
__global__ void k_post(unsigned long long* p) {
  const unsigned long long t = __builtin_amdgcn_s_memrealtime();
  unsigned d = 0xffffffffu;
  for (int i = 0; i < 16; ++i) { const unsigned long long e = p[64 + (threadIdx.x * 16 + i) * 16]; d = min(d, (unsigned)(t - e)); }
  for (int o = 32; o; o >>= 1) d = min(d, (unsigned)__shfl_xor((int)d, o));
  const unsigned long long until = t + (unsigned long long)d * POSTF;
  if (d < 100000) while (__builtin_amdgcn_s_memrealtime() < until) __builtin_amdgcn_s_sleep(2);
}

extern "C" void kernel_launch(void* const* d_in, const int* in_sizes, int n_in, void* d_out, int out_size, void* d_ws,
                              size_t ws_size, hipStream_t stream) {
  (void)hipFuncSetAttribute((const void*)mixer_kernel, hipFuncAttributeMaxDynamicSharedMemorySize, LDS_BYTES);
  k_pre<<<1,64,0,stream>>>((unsigned long long*)((char*)d_ws + (32<<20)));
  mixer_kernel<<<256, 256, LDS_BYTES, stream>>>((const unsigned short*)d_in[0], (const unsigned short*)d_in[1],
                                                (const unsigned short*)d_in[2], (const unsigned short*)d_in[3],
                                                (const unsigned short*)d_in[4], (unsigned short*)d_out, (unsigned char*)d_ws);
  k_post<<<1,64,0,stream>>>((unsigned long long*)((char*)d_ws + (32<<20)));
}
```

```cpp
#include <hip/hip_runtime.h>
#include <hip/hip_bf16.h>
#include <stdint.h>

#define PROFILE 0

typedef float f32x16 __attribute__((ext_vector_type(16)));
typedef float f32x2 __attribute__((ext_vector_type(2)));
typedef int i32x4 __attribute__((ext_vector_type(4)));
typedef unsigned u32x4 __attribute__((ext_vector_type(4)));
typedef unsigned u32x2 __attribute__((ext_vector_type(2)));
typedef __bf16 bf16x2 __attribute__((ext_vector_type(2)));
#define GAS __attribute__((address_space(1)))
#define LAS __attribute__((address_space(3)))

constexpr int S = 2048, D = 512, F = 1376;
constexpr int P1 = F * 2, P2 = S * 2;
constexpr int WS_E = 0, WS_R = 4096, WS_HF = 8192, WS_GF = 12288, WS_CTRL = 32768;
constexpr int WS_DBG = 65536;
constexpr int WS_H = 1 << 20, H_TILE = 128 * 2048;
constexpr int WS_G = 4 << 20, G_TILE = 86 * 2048;
constexpr int L_XCH = 0, L_OUT = 49152, L_RING = 65536, RING_W = 24576, LDS_BYTES = 163840;
constexpr int NHELP = 10;
constexpr unsigned SPIN_CAP = 1u << 20;

#define ACLOB10(p) "a" #p "0", "a" #p "1", "a" #p "2", "a" #p "3", "a" #p "4", "a" #p "5", "a" #p "6", "a" #p "7", "a" #p "8", "a" #p "9"
#define ACLOB_ALL "a0","a1","a2","a3","a4","a5","a6","a7","a8","a9", ACLOB10(1), ACLOB10(2), ACLOB10(3), ACLOB10(4), ACLOB10(5), ACLOB10(6), ACLOB10(7), ACLOB10(8), ACLOB10(9), \
  ACLOB10(10), ACLOB10(11), ACLOB10(12), ACLOB10(13), ACLOB10(14), ACLOB10(15), ACLOB10(16), ACLOB10(17), ACLOB10(18), ACLOB10(19), ACLOB10(20), ACLOB10(21), ACLOB10(22), ACLOB10(23), ACLOB10(24), \
  "a250","a251","a252","a253","a254","a255"
#define VCLOB10(p) "v" #p "0", "v" #p "1", "v" #p "2", "v" #p "3", "v" #p "4", "v" #p "5", "v" #p "6", "v" #p "7", "v" #p "8", "v" #p "9"
#define VCLOB_ALL VCLOB10(8), VCLOB10(9), VCLOB10(10), VCLOB10(11), VCLOB10(12), VCLOB10(13), VCLOB10(14), VCLOB10(15), VCLOB10(16), VCLOB10(17), VCLOB10(18), VCLOB10(19), VCLOB10(20)

__device__ __forceinline__ i32x4 make_srd(const void* p, unsigned bytes) {
  const uint64_t a = (uint64_t)p;
  return i32x4{(int)(uint32_t)a, (int)(uint32_t)(a >> 32), (int)bytes, 0x00020000};
}
__device__ __forceinline__ float asf(unsigned u) { return __builtin_bit_cast(float, u); }
__device__ __forceinline__ unsigned asu(float f) { return __builtin_bit_cast(unsigned, f); }
__device__ __forceinline__ unsigned pk_bf16(float lo, float hi) {
  bf16x2 v = {(__bf16)lo, (__bf16)hi};
  return __builtin_bit_cast(unsigned, v);
}
template <int CTRL> __device__ __forceinline__ float dpp_add(float v) {
  return v + asf((unsigned)__builtin_amdgcn_update_dpp(0, (int)asu(v), CTRL, 0xf, 0xf, true));
}
__device__ __forceinline__ void reduce8(const float (&v)[8], float (&r)[2]) {
  float a[4];
#pragma unroll
  for (int i = 0; i < 4; ++i) {
    auto t = __builtin_amdgcn_permlane32_swap(asu(v[i]), asu(v[i + 4]), false, false);
    const unsigned t0 = t[0], t1 = t[1];
    a[i] = asf(t0) + asf(t1);
  }
#pragma unroll
  for (int i = 0; i < 2; ++i) {
    auto t = __builtin_amdgcn_permlane16_swap(asu(a[i]), asu(a[i + 2]), false, false);
    const unsigned t0 = t[0], t1 = t[1];
    float s = asf(t0) + asf(t1);
    s = dpp_add<0x128>(s); s = dpp_add<0x124>(s); s = dpp_add<0x122>(s); s = dpp_add<0x121>(s);
    r[i] = s;
  }
}
__device__ __forceinline__ float bcast(const float (&r)[2], int row) {
  return asf((unsigned)__builtin_amdgcn_readlane((int)asu(r[row & 1]), (row >> 1) * 16));
}

#define STAMP_ASM(n) ""
#define STAMP(i) do {} while (0)

#define DMA_GROUP(V0, V1, V2, V3, SRD, SO, STEP, END, START) \
  "s_add_u32 m0, %[ring], slot*4096\n" \
  "s_nop 0\n" \
  "buffer_load_dwordx4 " V0 ", " SRD ", " SO " offen lds\n" \
  "buffer_load_dwordx4 " V1 ", " SRD ", " SO " offen offset:1024 lds\n" \
  "buffer_load_dwordx4 " V2 ", " SRD ", " SO " offen offset:2048 lds\n" \
  "buffer_load_dwordx4 " V3 ", " SRD ", " SO " offen offset:3072 lds\n" \
  "s_add_u32 " SO ", " SO ", " STEP "\n" \
  "s_cmp_eq_u32 " SO ", " END "\n" \
  "s_cselect_b32 " SO ", " START ", " SO "\n"
#define DMA_W1 DMA_GROUP("%[vw0]", "%[vw1]", "%[vw2]", "%[vw3]", "%[srdw]", "%[sow]", "32*2752", "%[sowe]", "%[sows]")
#define DMA_W2 DMA_GROUP("%[vx0]", "%[vx1]", "%[vx2]", "%[vx3]", "%[srdx]", "%[sox]", "32*4096", "%[soxe]", "%[soxs]")
#define TR_GROUP(TOFF) \
  "ds_read_b64_tr_b16 a[16*g+0:16*g+1], %[tr0] offset:(g%%6)*4096\n" \
  "ds_read_b64_tr_b16 a[16*g+2:16*g+3], %[tr0] offset:(g%%6)*4096+" TOFF "\n" \
  "ds_read_b64_tr_b16 a[16*g+4:16*g+5], %[tr1] offset:(g%%6)*4096\n" \
  "ds_read_b64_tr_b16 a[16*g+6:16*g+7], %[tr1] offset:(g%%6)*4096+" TOFF "\n" \
  "ds_read_b64_tr_b16 a[16*g+8:16*g+9], %[tr0] offset:(g%%6)*4096+2048\n" \
  "ds_read_b64_tr_b16 a[16*g+10:16*g+11], %[tr0] offset:(g%%6)*4096+2048+" TOFF "\n" \
  "ds_read_b64_tr_b16 a[16*g+12:16*g+13], %[tr1] offset:(g%%6)*4096+2048\n" \
  "ds_read_b64_tr_b16 a[16*g+14:16*g+15], %[tr1] offset:(g%%6)*4096+2048+" TOFF "\n"
#define ACT_LOAD(ST) \
  "buffer_load_dwordx4 v[128+16*sb+8*" ST ":131+16*sb+8*" ST "], %[va], %[srda], %[soa] offen offset:" ST "*2048 sc1\n" \
  "buffer_load_dwordx4 v[132+16*sb+8*" ST ":135+16*sb+8*" ST "], %[vb], %[srda], %[soa] offen offset:" ST "*2048 sc1\n"
#define ACT_STEP \
  "s_add_u32 %[soa], %[soa], 4096\n" \
  "s_cmp_eq_u32 %[soa], %[soae]\n" \
  "s_cselect_b32 %[soa], %[soas], %[soa]\n"
#define POLL(L) \
  "s_mov_b32 %[cnt], 0x100000\n" \
  L "0%=:\n" \
  "buffer_load_dword v192, %[vfl], %[srdc], 0 offen sc1\n" \
  "s_waitcnt vmcnt(0)\n" \
  "v_cmp_ne_u32 vcc, %[tag], v192\n" \
  "s_and_b64 vcc, vcc, %[pmask]\n" \
  "s_cbranch_scc0 " L "1%=\n" \
  "s_sub_u32 %[cnt], %[cnt], 1\n" \
  "s_cbranch_scc0 " L "0%=\n" \
  L "1%=:\n"
#define XCHG \
  "s_nop 7\n s_nop 7\n" \
  ".set r, 0\n .rept 4\n ds_write_b128 %[xw1], v[80+4*r:83+4*r] offset:r*1024\n .set r, r+1\n .endr\n" \
  ".set r, 0\n .rept 4\n ds_write_b128 %[xw2], v[96+4*r:99+4*r] offset:4096+r*1024\n .set r, r+1\n .endr\n" \
  ".set r, 0\n .rept 4\n ds_write_b128 %[xw3], v[112+4*r:115+4*r] offset:8192+r*1024\n .set r, r+1\n .endr\n" \
  "s_waitcnt lgkmcnt(0)\n" \
  "s_barrier\n" \
  ".set r, 0\n .rept 12\n ds_read_b128 v[80+4*r:83+4*r], %[xr] offset:r*1024\n .set r, r+1\n .endr\n" \
  "s_waitcnt lgkmcnt(8)\n" \
  ".set r, 0\n .rept 8\n v_pk_add_f32 v[64+2*r:65+2*r], v[64+2*r:65+2*r], v[80+2*r:81+2*r]\n .set r, r+1\n .endr\n" \
  "s_waitcnt lgkmcnt(4)\n" \
  ".set r, 0\n .rept 8\n v_pk_add_f32 v[64+2*r:65+2*r], v[64+2*r:65+2*r], v[96+2*r:97+2*r]\n .set r, r+1\n .endr\n" \
  "s_waitcnt lgkmcnt(0)\n" \
  ".set r, 0\n .rept 8\n v_pk_add_f32 v[64+2*r:65+2*r], v[64+2*r:65+2*r], v[112+2*r:113+2*r]\n .set r, r+1\n .endr\n"

__global__ __launch_bounds__(256, 1) void mixer_kernel(const unsigned short* __restrict__ x, const unsigned short* __restrict__ w1,
                                                       const unsigned short* __restrict__ b1, const unsigned short* __restrict__ w2,
                                                       const unsigned short* __restrict__ b2, unsigned short* __restrict__ out,
                                                       unsigned char* __restrict__ ws) {
  extern __shared__ __attribute__((aligned(16))) unsigned char lds[];
  STAMP(0);
  const int lane = threadIdx.x & 63;
  const int w = __builtin_amdgcn_readfirstlane(threadIdx.x >> 6);
  const int b = blockIdx.x, xc = b & 7, j = b >> 3, half = xc & 1, q = xc >> 1;
  const unsigned ldsb = (unsigned)(uintptr_t)(LAS unsigned char*)lds;
  const i32x4 srdc = make_srd(ws, WS_CTRL);
  const __amdgpu_buffer_rsrc_t rsc = __builtin_amdgcn_make_buffer_rsrc(ws, 0, 8 << 20, 0x00020000);

  const int hh = lane >> 5, blk = (lane >> 4) & 1, tq = (lane & 15) >> 2, tp = lane & 3;
  const unsigned ring = ldsb + L_RING + w * RING_W;
  const unsigned trc = ring + 32 * blk + 8 * tp;
  const unsigned sw0 = 64 * ((w >> 1) ^ (tq >> 1)), sw1 = sw0 ^ 64;
  const unsigned va = lane * 16 + (w & 1) * 1024, vb = va ^ 1024;
  const unsigned chunk = ((lane & 7) ^ (((lane >> 4) & 1) << 2)) * 16;
  const unsigned xw1 = ldsb + L_XCH + (w ^ 1) * 12288 + lane * 16, xw2 = ldsb + L_XCH + (w ^ 2) * 12288 + lane * 16,
                 xw3 = ldsb + L_XCH + (w ^ 3) * 12288 + lane * 16, xr = ldsb + L_XCH + w * 12288 + lane * 16;
  const int st = q * 8 + (j & 7), dt2 = half * 4 + (j >> 3);
  const i32x4 srdx = make_srd(w2 + st * 64, (unsigned)(F * P2 - st * 128));
  const unsigned vx0 = (lane >> 3) * P2 + chunk, vx1 = vx0 + 8 * P2 - 1024, vx2 = vx0 + 16 * P2 - 2048, vx3 = vx0 + 24 * P2 - 3072;
  const int rot2 = ((j >> 3) & 1) ^ (j & 1) ? 5 : 0;
  const unsigned soxs = (unsigned)w * (352 * P2), soxe = soxs + 352 * P2;
  unsigned sox = soxs + rot2 * (32 * P2);
  unsigned tag;

  if (j < NHELP) {
    if (j < 8) {
      const int u = (xc * 8 + j) * 4 + w;
      u32x4 xv[8];
#pragma unroll
      for (int i = 0; i < 8; ++i) xv[i] = *(const GAS u32x4*)((const GAS unsigned char*)x + (size_t)(8 * u + i) * 1024 + lane * 16);
      const unsigned e = __builtin_amdgcn_raw_buffer_load_b32(rsc, WS_E, 0, 16);
      float s8[8];
      const bf16x2 ones = {(__bf16)1.0f, (__bf16)1.0f};
#pragma unroll
      for (int i = 0; i < 8; ++i) {
        float s = 0.f;
#pragma unroll
        for (int c = 0; c < 4; ++c) { const unsigned t = xv[i][c]; s = __builtin_amdgcn_fdot2_f32_bf16(__builtin_bit_cast(bf16x2, t), ones, s, false); }
        s8[i] = s;
      }
      STAMP(1);
      float rm[2];
      reduce8(s8, rm);
      rm[0] *= (1.0f / D); rm[1] *= (1.0f / D);
      f32x2 dv[4][8];
      float q8[8];
#pragma unroll
      for (int p = 0; p < 4; ++p) {
        const f32x2 m = {bcast(rm, 2 * p), bcast(rm, 2 * p + 1)};
        f32x2 qq = {0.f, 0.f};
#pragma unroll
        for (int c = 0; c < 8; ++c) {
          const unsigned t0 = xv[2 * p][c >> 1], t1 = xv[2 * p + 1][c >> 1];
          f32x2 v = (c & 1) ? f32x2{asf(t0 & 0xffff0000u), asf(t1 & 0xffff0000u)} : f32x2{asf(t0 << 16), asf(t1 << 16)};
          v -= m;
          dv[p][c] = v;
          qq += v * v;
        }
        q8[2 * p] = qq[0]; q8[2 * p + 1] = qq[1];
      }
      float rv[2];
      reduce8(q8, rv);
      rv[0] = __builtin_amdgcn_rsqf(rv[0] * (1.0f / D) + 1e-5f);
      rv[1] = __builtin_amdgcn_rsqf(rv[1] * (1.0f / D) + 1e-5f);
      f32x2 rs[4];
#pragma unroll
      for (int p = 0; p < 4; ++p) rs[p] = f32x2{bcast(rv, 2 * p), bcast(rv, 2 * p + 1)};
      const unsigned ho = WS_H + (lane >> 3) * H_TILE + (u >> 1) * 2048 + (u & 1) * 512 + (lane & 7) * 16;
#pragma unroll
      for (int c = 0; c < 8; ++c) {
        u32x4 o;
#pragma unroll
        for (int p = 0; p < 4; ++p) { const f32x2 v = dv[p][c] * rs[p]; o[p] = pk_bf16(v[0], v[1]); }
        __builtin_amdgcn_raw_buffer_store_b128(o, rsc, ho + (c >> 2) * 1024 + (c & 3) * 128, 0, 16);
      }
      STAMP(2);
      tag = __builtin_amdgcn_readfirstlane(e) * 0x9E3779B1u + 0x7F4A7C15u;
      asm volatile("s_waitcnt vmcnt(0)" ::: "memory");
      if (lane == 0) __builtin_amdgcn_raw_buffer_store_b32(tag, rsc, WS_HF + u * 4, 0, 16);
      STAMP(3);
    } else {
      const unsigned e = __builtin_amdgcn_raw_buffer_load_b32(rsc, WS_E, 0, 16);
      tag = __builtin_amdgcn_readfirstlane(e) * 0x9E3779B1u + 0x7F4A7C15u;
    }
    if (lane == 0) __builtin_amdgcn_raw_buffer_store_b32(tag, rsc, WS_R + (b * 4 + w) * 4, 0, 16);
    if (b == 9 * 8) {
      for (unsigned it = 0; it < SPIN_CAP; ++it) {
        const u32x4 v = __builtin_amdgcn_raw_buffer_load_b128(rsc, WS_R + (w * 64 + lane) * 16, 0, 16);
        if (__all(v[0] == tag && v[1] == tag && v[2] == tag && v[3] == tag)) break;
        __builtin_amdgcn_s_sleep(4);
      }
      __builtin_amdgcn_s_barrier();
      if (threadIdx.x == 0) __builtin_amdgcn_raw_buffer_store_b32(tag, rsc, WS_E, 0, 16);
    }
    asm volatile(
        ".set slot, 0\n .rept 6\n" DMA_W2 ".set slot, slot+1\n .endr\n"
        : [sox] "+s"(sox)
        : [ring] "s"(ring), [vx0] "v"(vx0), [vx1] "v"(vx1), [vx2] "v"(vx2), [vx3] "v"(vx3), [srdx] "s"(srdx), [soxe] "s"(soxe), [soxs] "s"(soxs)
        : "memory", "scc");
  } else {
    const int idx = q * 22 + (j - NHELP), ft = idx >> 2, dt = half * 4 + (idx & 3);
    const int f0 = ft < 21 ? ft * 64 : F - 64;
    const i32x4 srdw = make_srd(w1 + f0, (unsigned)(S * P1 - f0 * 2));
    const i32x4 srda = make_srd(ws + WS_H + dt * H_TILE, H_TILE);
    const i32x4 srdb = make_srd(b1 + f0, 128);
    const unsigned vw0 = (lane >> 3) * P1 + chunk, vw1 = vw0 + 8 * P1 - 1024, vw2 = vw0 + 16 * P1 - 2048, vw3 = vw0 + 24 * P1 - 3072;
    const int rot = (((idx & 3) + ft) & 3) * 4 + half * 2;
    const unsigned sows = (unsigned)w * (512 * P1), sowe = sows + 512 * P1;
    unsigned sow = sows + rot * (32 * P1);
    const unsigned soas = (unsigned)w * 65536u, soae = soas + 65536u;
    unsigned soa = soas + rot * 4096;
    const unsigned tr0 = trc + (8 * hh + tq) * 128 + sw0, tr1 = trc + (8 * hh + tq) * 128 + sw1;
    const unsigned vfl = WS_HF + (w * 64 + lane) * 4;
    const unsigned vbias = (w >> 1) * 64 + hh * 8;
    const unsigned vr = WS_R + (b * 4 + w) * 4;
    const unsigned long long pmask = ~0ull;
    unsigned cnt;
    f32x16 acc;
    asm volatile(
        "buffer_load_dword v193, off, %[srdc], 0 sc1\n"
        "buffer_load_dwordx2 v[194:195], %[vbias], %[srdb], 0 offen\n"
        "buffer_load_dwordx2 v[196:197], %[vbias], %[srdb], 0 offen offset:16\n"
        "buffer_load_dwordx2 v[198:199], %[vbias], %[srdb], 0 offen offset:32\n"
        "buffer_load_dwordx2 v[200:201], %[vbias], %[srdb], 0 offen offset:48\n"
        ".set slot, 0\n .rept 5\n" DMA_W1 ".set slot, slot+1\n .endr\n"
        "s_waitcnt vmcnt(20)\n"
        "v_readfirstlane_b32 %[tag], v193\n"
        ".set r, 0\n .rept 8\n v_lshlrev_b32 v[64+2*r], 16, v[194+r]\n v_and_b32 v[65+2*r], 0xffff0000, v[194+r]\n .set r, r+1\n .endr\n"
        "s_mul_i32 %[tag], %[tag], 0x9E3779B1\n"
        "s_add_u32 %[tag], %[tag], 0x7F4A7C15\n"
        "v_mov_b32 v193, %[tag]\n"
        "buffer_store_dword v193, %[vr], %[srdc], 0 offen sc1\n"
        ".set g, 0\n .rept 11\n"
        "s_waitcnt vmcnt(16)\n"
        TR_GROUP("512")
        "s_waitcnt lgkmcnt(8)\n"
        ".set slot, (g+5)%%6\n"
        DMA_W1
        ".set g, g+1\n .endr\n"
        STAMP_ASM(1)
        POLL(".Lp")
        STAMP_ASM(2)
        ".set sb, 0\n .rept 4\n" ACT_LOAD("0") ACT_LOAD("1") ACT_STEP ".set sb, sb+1\n .endr\n"
        ".rept 5\n"
        TR_GROUP("512")
        "s_waitcnt lgkmcnt(8)\n"
        ".set g, g+1\n .endr\n"
        "s_waitcnt lgkmcnt(0)\n"
        ".set g, 0\n .rept 16\n"
        ".set sb, g%%4\n"
        "s_waitcnt vmcnt(14)\n"
        ".if g == 0\n"
        "v_mfma_f32_32x32x16_bf16 v[64:79], a[16*g:16*g+3], v[128+16*sb:131+16*sb], v[64:79]\n"
        "v_mfma_f32_32x32x16_bf16 v[80:95], a[16*g:16*g+3], v[132+16*sb:135+16*sb], 0\n"
        "v_mfma_f32_32x32x16_bf16 v[96:111], a[16*g+4:16*g+7], v[128+16*sb:131+16*sb], 0\n"
        "v_mfma_f32_32x32x16_bf16 v[112:127], a[16*g+4:16*g+7], v[132+16*sb:135+16*sb], 0\n"
        ".else\n"
        "v_mfma_f32_32x32x16_bf16 v[64:79], a[16*g:16*g+3], v[128+16*sb:131+16*sb], v[64:79]\n"
        "v_mfma_f32_32x32x16_bf16 v[80:95], a[16*g:16*g+3], v[132+16*sb:135+16*sb], v[80:95]\n"
        "v_mfma_f32_32x32x16_bf16 v[96:111], a[16*g+4:16*g+7], v[128+16*sb:131+16*sb], v[96:111]\n"
        "v_mfma_f32_32x32x16_bf16 v[112:127], a[16*g+4:16*g+7], v[132+16*sb:135+16*sb], v[112:127]\n"
        ".endif\n"
        ".if g < 12\n" ACT_LOAD("0") "s_waitcnt vmcnt(14)\n" ".else\n" "s_waitcnt vmcnt(12)\n" ".endif\n"
        "v_mfma_f32_32x32x16_bf16 v[80:95], a[16*g+8:16*g+11], v[140+16*sb:143+16*sb], v[80:95]\n"
        "v_mfma_f32_32x32x16_bf16 v[96:111], a[16*g+12:16*g+15], v[136+16*sb:139+16*sb], v[96:111]\n"
        "v_mfma_f32_32x32x16_bf16 v[112:127], a[16*g+12:16*g+15], v[140+16*sb:143+16*sb], v[112:127]\n"
        "v_mfma_f32_32x32x16_bf16 v[64:79], a[16*g+8:16*g+11], v[136+16*sb:139+16*sb], v[64:79]\n"
        ".if g < 12\n" ACT_LOAD("1") ACT_STEP ".else\n" ".set slot, g-12\n" DMA_W2 ".endif\n"
        ".set g, g+1\n .endr\n"
        ".set slot, 4\n" DMA_W2 ".set slot, 5\n" DMA_W2
        STAMP_ASM(3)
        XCHG
        : "=&{v[64:79]}"(acc), [sow] "+s"(sow), [soa] "+s"(soa), [sox] "+s"(sox), [cnt] "=&s"(cnt), [tag] "=&s"(tag)
        : [ring] "s"(ring), [vw0] "v"(vw0), [vw1] "v"(vw1), [vw2] "v"(vw2), [vw3] "v"(vw3), [srdw] "s"(srdw), [sowe] "s"(sowe), [sows] "s"(sows),
          [vx0] "v"(vx0), [vx1] "v"(vx1), [vx2] "v"(vx2), [vx3] "v"(vx3), [srdx] "s"(srdx), [soxe] "s"(soxe), [soxs] "s"(soxs),
          [tr0] "v"(tr0), [tr1] "v"(tr1), [va] "v"(va), [vb] "v"(vb), [srda] "s"(srda), [soae] "s"(soae), [soas] "s"(soas),
          [vfl] "v"(vfl), [srdc] "s"(srdc), [pmask] "s"(pmask), [vbias] "v"(vbias), [srdb] "s"(srdb), [vr] "v"(vr),
          [xw1] "v"(xw1), [xw2] "v"(xw2), [xw3] "v"(xw3), [xr] "v"(xr)
        : "memory", "scc", "vcc", VCLOB_ALL, ACLOB_ALL);
    STAMP(4);
    u32x4 o[2];
#pragma unroll
    for (int r = 0; r < 8; ++r) {
      const f32x2 u = {acc[2 * r], acc[2 * r + 1]};
      constexpr float k0 = -2.0f * 0.7978845608028654f * 1.4426950408889634f, k1 = k0 * 0.044715f;
      const f32x2 t = u * (u * u * k1 + k0);
      const f32x2 dn = f32x2{__builtin_amdgcn_exp2f(t[0]), __builtin_amdgcn_exp2f(t[1])} + 1.0f;
      const f32x2 g = u * f32x2{__builtin_amdgcn_rcpf(dn[0]), __builtin_amdgcn_rcpf(dn[1])};
      o[r >> 2][r & 3] = pk_bf16(g[0], g[1]);
    }
    const int f32t = (f0 >> 5) + (w >> 1);
    if (ft < 21 || w >= 2) {
      const unsigned go = WS_G + dt * G_TILE + f32t * 4096 + (w & 1) * 1024 + lane * 16;
      __builtin_amdgcn_raw_buffer_store_b128(o[0], rsc, go, 0, 16);
      __builtin_amdgcn_raw_buffer_store_b128(o[1], rsc, go + 2048, 0, 16);
      STAMP(5);
      asm volatile("s_waitcnt vmcnt(0)" ::: "memory");
      if (lane == 0) __builtin_amdgcn_raw_buffer_store_b32(tag, rsc, WS_GF + (dt * 128 + f32t * 2 + (w & 1)) * 4, 0, 16);
    }
  }
  STAMP(6);
  {
    const i32x4 srda = make_srd(ws + WS_G + dt2 * G_TILE, G_TILE);
    const i32x4 srdb = make_srd(b2 + st * 64, 128);
    const unsigned soas = (unsigned)w * (11 * 4096), soae = soas + 11 * 4096;
    unsigned soa = soas + rot2 * 4096;
    const unsigned tr0 = trc + (4 * hh + tq) * 128 + sw0, tr1 = trc + (4 * hh + tq) * 128 + sw1;
    const unsigned vfl = WS_GF + (dt2 * 128 + w * 22 + lane) * 4;
    const unsigned long long pmask = w < 3 ? (1ull << 22) - 1 : (1ull << 20) - 1;
    const unsigned vbias = (w >> 1) * 64 + (lane & 31) * 2;
    unsigned cnt;
    f32x16 acc;
    asm volatile(
        "buffer_load_ushort v194, %[vbias], %[srdb], 0 offen\n"
        ".set g, 0\n .rept 6\n"
        ".if g == 0\n s_waitcnt vmcnt(21)\n .else\n s_waitcnt vmcnt(16)\n .endif\n"
        TR_GROUP("1024")
        ".if g > 0\n"
        "s_waitcnt lgkmcnt(8)\n"
        ".set slot, (g+5)%%6\n"
        DMA_W2
        ".endif\n"
        ".set g, g+1\n .endr\n"
        "s_barrier\n"
        STAMP_ASM(7)
        POLL(".Lq")
        STAMP_ASM(8)
        ".set sb, 0\n .rept 4\n" ACT_LOAD("0") ACT_LOAD("1") ACT_STEP ".set sb, sb+1\n .endr\n"
        "v_lshlrev_b32 v194, 16, v194\n"
        ".rept 5\n"
        TR_GROUP("1024")
        "s_waitcnt lgkmcnt(8)\n"
        ".set g, g+1\n .endr\n"
        ".set r, 0\n .rept 16\n v_mov_b32 v[64+r], v194\n .set r, r+1\n .endr\n"
        "s_waitcnt lgkmcnt(0)\n"
        ".set g, 0\n .rept 11\n"
        ".set sb, g%%4\n"
        ".if g < 8\n s_waitcnt vmcnt(14)\n .else\n s_waitcnt vmcnt(4*(10-g)+2)\n .endif\n"
        ".if g == 0\n"
        "v_mfma_f32_32x32x16_bf16 v[64:79], v[128+16*sb:131+16*sb], a[16*g:16*g+3], v[64:79]\n"
        "v_mfma_f32_32x32x16_bf16 v[80:95], v[132+16*sb:135+16*sb], a[16*g:16*g+3], 0\n"
        "v_mfma_f32_32x32x16_bf16 v[96:111], v[128+16*sb:131+16*sb], a[16*g+4:16*g+7], 0\n"
        "v_mfma_f32_32x32x16_bf16 v[112:127], v[132+16*sb:135+16*sb], a[16*g+4:16*g+7], 0\n"
        ".else\n"
        "v_mfma_f32_32x32x16_bf16 v[64:79], v[128+16*sb:131+16*sb], a[16*g:16*g+3], v[64:79]\n"
        "v_mfma_f32_32x32x16_bf16 v[80:95], v[132+16*sb:135+16*sb], a[16*g:16*g+3], v[80:95]\n"
        "v_mfma_f32_32x32x16_bf16 v[96:111], v[128+16*sb:131+16*sb], a[16*g+4:16*g+7], v[96:111]\n"
        "v_mfma_f32_32x32x16_bf16 v[112:127], v[132+16*sb:135+16*sb], a[16*g+4:16*g+7], v[112:127]\n"
        ".endif\n"
        ".if g < 7\n" ACT_LOAD("0") "s_waitcnt vmcnt(14)\n" ".else\n" "s_waitcnt vmcnt(4*(10-g))\n" ".endif\n"
        "v_mfma_f32_32x32x16_bf16 v[80:95], v[140+16*sb:143+16*sb], a[16*g+8:16*g+11], v[80:95]\n"
        "v_mfma_f32_32x32x16_bf16 v[96:111], v[136+16*sb:139+16*sb], a[16*g+12:16*g+15], v[96:111]\n"
        "v_mfma_f32_32x32x16_bf16 v[112:127], v[140+16*sb:143+16*sb], a[16*g+12:16*g+15], v[112:127]\n"
        "v_mfma_f32_32x32x16_bf16 v[64:79], v[136+16*sb:139+16*sb], a[16*g+8:16*g+11], v[64:79]\n"
        ".if g < 7\n" ACT_LOAD("1") ACT_STEP ".endif\n"
        ".set g, g+1\n .endr\n"
        STAMP_ASM(9)
        XCHG
        : "=&{v[64:79]}"(acc), [soa] "+s"(soa), [sox] "+s"(sox), [cnt] "=&s"(cnt)
        : [ring] "s"(ring), [vx0] "v"(vx0), [vx1] "v"(vx1), [vx2] "v"(vx2), [vx3] "v"(vx3), [srdx] "s"(srdx), [soxe] "s"(soxe), [soxs] "s"(soxs),
          [tr0] "v"(tr0), [tr1] "v"(tr1), [va] "v"(va), [vb] "v"(vb), [srda] "s"(srda), [soae] "s"(soae), [soas] "s"(soas),
          [vfl] "v"(vfl), [srdc] "s"(srdc), [pmask] "s"(pmask), [vbias] "v"(vbias), [srdb] "s"(srdb), [tag] "s"(tag),
          [xw1] "v"(xw1), [xw2] "v"(xw2), [xw3] "v"(xw3), [xr] "v"(xr)
        : "memory", "scc", "vcc", VCLOB_ALL, ACLOB_ALL);
    STAMP(10);
    LAS unsigned char* ot = (LAS unsigned char*)lds + L_OUT;
    {
      const unsigned o = ((w >> 1) * 32 + (lane & 31)) * 144 + (32 * hh + 4 * (w & 1)) * 2;
#pragma unroll
      for (int e = 0; e < 4; ++e)
        *(LAS u32x2*)(ot + o + e * 16) = u32x2{pk_bf16(acc[e], acc[4 + e]), pk_bf16(acc[8 + e], acc[12 + e])};
    }
    __syncthreads();
#pragma unroll
    for (int i = 0; i < 2; ++i) {
      const int row = w * 16 + i * 8 + (lane >> 3);
      const u32x4 v = *(LAS u32x4*)(ot + row * 144 + (lane & 7) * 16);
      *(GAS u32x4*)((GAS unsigned char*)out + (size_t)(st * 64 + row) * 1024 + dt2 * 128 + (lane & 7) * 16) = v;
    }
  }
}

__global__ __launch_bounds__(256,1) void k_probe(unsigned* p) {
  unsigned long long ta, tb, v; auto ka = __builtin_amdgcn_kernarg_segment_ptr();
  asm volatile("s_memrealtime %0\n s_waitcnt lgkmcnt(0)\n s_load_dwordx2 %2, %3, 0\n s_waitcnt lgkmcnt(0)\n s_memrealtime %1\n s_waitcnt lgkmcnt(0)" : "=&s"(ta), "=&s"(tb), "=&s"(v) : "s"(ka));
  const unsigned long long until = tb + (tb - ta) * 0;
  while (__builtin_amdgcn_s_memrealtime() < until) __builtin_amdgcn_s_sleep(2);
}
extern "C" void kernel_launch(void* const* d_in, const int* in_sizes, int n_in, void* d_out, int out_size, void* d_ws,
                              size_t ws_size, hipStream_t stream) {
  (void)hipFuncSetAttribute((const void*)mixer_kernel, hipFuncAttributeMaxDynamicSharedMemorySize, LDS_BYTES);
  k_probe<<<256,256,0,stream>>>((unsigned*)d_ws);
  mixer_kernel<<<256, 256, LDS_BYTES, stream>>>((const unsigned short*)d_in[0], (const unsigned short*)d_in[1],
                                                (const unsigned short*)d_in[2], (const unsigned short*)d_in[3],
                                                (const unsigned short*)d_in[4], (unsigned short*)d_out, (unsigned char*)d_ws);
}
```

```cpp
#include <hip/hip_runtime.h>
#include <hip/hip_bf16.h>
#include <stdint.h>

#define PROFILE 0

typedef float f32x16 __attribute__((ext_vector_type(16)));
typedef float f32x2 __attribute__((ext_vector_type(2)));
typedef int i32x4 __attribute__((ext_vector_type(4)));
typedef unsigned long long u64x8 __attribute__((ext_vector_type(8)));
typedef unsigned u32x4 __attribute__((ext_vector_type(4)));
typedef unsigned u32x2 __attribute__((ext_vector_type(2)));
typedef __bf16 bf16x2 __attribute__((ext_vector_type(2)));
#define GAS __attribute__((address_space(1)))
#define LAS __attribute__((address_space(3)))

constexpr int S = 2048, D = 512, F = 1376;
constexpr int P1 = F * 2, P2 = S * 2;
constexpr int WS_E = 0, WS_FL = 4096, WS_FL2 = 36864, WS_CTRL = 49152;
constexpr int WS_DBG = 65536;
constexpr int WS_H = 1 << 20, H_TILE = 128 * 2048;
constexpr int WS_G = 4 << 20, G_TILE = 86 * 2048;
constexpr int WS_X = 6 << 20;
constexpr int L_XCH = 0, L_KEEP = 36864, L_XCH2 = 16384, L_OUT = 0, L_RING = 73728, RING_W = 20480, L_LN = 155648, LDS_BYTES = 163840;
constexpr int LIGHT = 2;
constexpr unsigned SPIN_CAP = 1u << 20;

#define C10(r, p) r #p "0", r #p "1", r #p "2", r #p "3", r #p "4", r #p "5", r #p "6", r #p "7", r #p "8", r #p "9"
#define C80_249(r) C10(r, 8), C10(r, 9), C10(r, 10), C10(r, 11), C10(r, 12), C10(r, 13), C10(r, 14), C10(r, 15), C10(r, 16), \
  C10(r, 17), C10(r, 18), C10(r, 19), C10(r, 20), C10(r, 21), C10(r, 22), C10(r, 23), C10(r, 24)
#define ACLOB_ALL C10("a", ), C10("a", 1), C10("a", 2), C10("a", 3), C10("a", 4), C10("a", 5), C10("a", 6), C10("a", 7), C80_249("a"), \
  "a250", "a251", "a252", "a253", "a254", "a255"
#define VCLOB_ALL C80_249("v"), "v250", "v251", "v252", "v253", "v254", "v255"

__device__ __forceinline__ i32x4 make_srd(uint64_t a, unsigned bytes) {
  return i32x4{(int)(uint32_t)a, (int)(uint32_t)(a >> 32), (int)bytes, 0x00020000};
}
__device__ __forceinline__ float asf(unsigned u) { return __builtin_bit_cast(float, u); }
__device__ __forceinline__ unsigned asu(float f) { return __builtin_bit_cast(unsigned, f); }
__device__ __forceinline__ unsigned pk_bf16(float lo, float hi) {
  bf16x2 v = {(__bf16)lo, (__bf16)hi};
  return __builtin_bit_cast(unsigned, v);
}
template <int CTRL> __device__ __forceinline__ float dpp_add(float v) {
  return v + asf((unsigned)__builtin_amdgcn_update_dpp(0, (int)asu(v), CTRL, 0xf, 0xf, true));
}
__device__ __forceinline__ float reduce2(float v0, float v1) {
  auto t = __builtin_amdgcn_permlane32_swap(asu(v0), asu(v1), false, false);
  const unsigned t0 = t[0], t1 = t[1];
  const float a = asf(t0) + asf(t1);
  auto u = __builtin_amdgcn_permlane16_swap(asu(a), asu(a), false, false);
  const unsigned u0 = u[0], u1 = u[1];
  float s = asf(u0) + asf(u1);
  s = dpp_add<0x128>(s); s = dpp_add<0x124>(s); s = dpp_add<0x122>(s); s = dpp_add<0x121>(s);
  return s;
}
__device__ __forceinline__ f32x2 both(float s) {
  return f32x2{asf((unsigned)__builtin_amdgcn_readlane((int)asu(s), 0)), asf((unsigned)__builtin_amdgcn_readlane((int)asu(s), 32))};
}

#define STAMP_ASM(n) ""
#define STAMP(i) do {} while (0)

__global__ __launch_bounds__(256, 1) void mixer_kernel(const void* a_x, const void* a_w1, const void* a_b1, const void* a_w2, const void* a_b2,
                                                       void* a_out, void* a_ws, unsigned long long a_pad) {
  extern __shared__ __attribute__((aligned(16))) unsigned char lds[];
  STAMP(0);
  u64x8 ka;
  {
    unsigned long long pc;
    asm volatile(
        "s_load_dwordx16 %[ka], %[kp], 0\n"
        "s_getpc_b64 %[pc]\n"
        ".Lcode_a:\n"
        "v_lshlrev_b32 v255, 7, %[tid]\n"
        "v_min_u32 v255, .Lcode_z-.Lcode_a, v255\n"
        "global_load_dword v255, v255, %[pc]\n"
        : [ka] "=&s"(ka), [pc] "=&s"(pc)
        : [kp] "s"(__builtin_amdgcn_kernarg_segment_ptr()), [tid] "v"(threadIdx.x)
        : "v255");
    __builtin_amdgcn_sched_barrier(0);
  }
  const int lane = threadIdx.x & 63;
  const int w = __builtin_amdgcn_readfirstlane(threadIdx.x >> 6);
  const int b = blockIdx.x, xc = b & 7, j = b >> 3, half = xc & 1, q = xc >> 1;
  const unsigned ldsb = (unsigned)(uintptr_t)(LAS unsigned char*)lds;
  const unsigned ring = ldsb + L_RING + w * RING_W;
  const int qi = q == LIGHT ? 3 : q == 3 ? LIGHT : q;
  const int ft = qi * 4 + (j >> 3), dt = j & 7;
  const int f0 = ft < 14 ? ft * 96 : F - 96;
  const int rot = ((dt >> 0) * 2 + ft * 0) & 15;
  const unsigned pb = (f0 >> 5) & 1, r0 = lane >> 3, r2 = ((lane >> 3) & 1) | ((lane >> 1) & 2) | ((lane >> 3) & 4) | ((lane >> 1) & 8);
  unsigned vw0 = 2048 + r0 * P1 + ((r0 + pb) & 1) * 64 + ((lane ^ (lane >> 2)) & 4) * 16 + (lane & 3) * 16, vw1 = vw0 + 8 * P1 - 1024;
  unsigned vw2 = r2 * P1 + ((r2 + pb + 1) & 1) * 128 + (lane & 3) * 16;
  unsigned sows = (unsigned)(half * 4 + w) * (256 * P1), sowe = sows + 256 * P1;
  unsigned sow = sows + rot * (16 * P1);
  unsigned vxl = (b * 8 + w * 2) * 1024 + lane * 16;
  asm volatile("s_waitcnt lgkmcnt(0)" : "+s"(ka), "+s"(sow), "+s"(sows), "+s"(sowe), "+v"(vw0), "+v"(vw1), "+v"(vw2), "+v"(vxl));
  const i32x4 srdw = make_srd(ka[1] + f0 * 2 - 2048, ft < 15 ? (unsigned)(S * P1 - f0 * 2 + 2048) : 0u);
  const i32x4 srdc = make_srd(ka[6], WS_CTRL);
  const unsigned goff = WS_G + dt * G_TILE + ((f0 >> 5) + w) * 4096 + half * 1024, gown = ((unsigned)(0x0477777777777777ull >> (4 * ft + w)) & 1u) << 12;
  const unsigned vl16 = lane * 16;
  u32x4 x0, x1;
  unsigned e, tmp;
  asm volatile(
        "global_load_dwordx4 %[x0], %[vxl], %[px]\n"
        "global_load_dwordx4 %[x1], %[vxl], %[px] offset:1024\n"
        "buffer_load_dword %[e], off, %[srdc], 0 sc1\n"
        "s_barrier\n"
        "s_add_u32 m0, %[ring], 0\n"
        "s_add_u32 %[tmp], %[sow], 44032\n"
        "buffer_load_dwordx4 %[vw0], %[srdw], %[sow] offen lds\n"
        "buffer_load_dwordx4 %[vw1], %[srdw], %[sow] offen offset:1024 lds\n"
        "buffer_load_dwordx4 %[vw2], %[srdw], %[sow] offen offset:2048 lds\n"
        "s_cmp_eq_u32 %[tmp], %[sowe]\n"
        "s_cselect_b32 %[sow], %[sows], %[tmp]\n"
        "s_add_u32 m0, %[ring], 3072\n"
        "s_add_u32 %[tmp], %[sow], 44032\n"
        "buffer_load_dwordx4 %[vw0], %[srdw], %[sow] offen lds\n"
        "buffer_load_dwordx4 %[vw1], %[srdw], %[sow] offen offset:1024 lds\n"
        "buffer_load_dwordx4 %[vw2], %[srdw], %[sow] offen offset:2048 lds\n"
        "s_cmp_eq_u32 %[tmp], %[sowe]\n"
        "s_cselect_b32 %[sow], %[sows], %[tmp]\n"
        "s_waitcnt vmcnt(6)\n"
      : [sow] "+s"(sow), [x0] "=&v"(x0), [x1] "=&v"(x1), [e] "=&v"(e), [tmp] "=&s"(tmp)
      : [vxl] "v"(vxl), [px] "s"(ka[0]), [srdc] "s"(srdc), [ring] "s"(ring), [vw0] "v"(vw0), [vw1] "v"(vw1), [vw2] "v"(vw2),
        [srdw] "s"(srdw), [sowe] "s"(sowe), [sows] "s"(sows)
      : "memory", "scc", "v255");
  STAMP(1);
  const unsigned tag = __builtin_amdgcn_readfirstlane(e) * 0x9E3779B1u + 0x7F4A7C15u;
  const uint64_t wsd = ka[6];
  const __amdgpu_buffer_rsrc_t rsc = __builtin_amdgcn_make_buffer_rsrc((void*)ka[6], 0, 4 << 20, 0x00020000);
  {
    const bf16x2 ones = {(__bf16)1.0f, (__bf16)1.0f};
    float s0 = 0.f, s1 = 0.f;
#pragma unroll
    for (int c = 0; c < 4; ++c) {
      const unsigned t0 = x0[c], t1 = x1[c];
      s0 = __builtin_amdgcn_fdot2_f32_bf16(__builtin_bit_cast(bf16x2, t0), ones, s0, false);
      s1 = __builtin_amdgcn_fdot2_f32_bf16(__builtin_bit_cast(bf16x2, t1), ones, s1, false);
    }
    const f32x2 m = both(reduce2(s0, s1) * (1.0f / D));
    f32x2 dv[8], qq = {0.f, 0.f};
#pragma unroll
    for (int c = 0; c < 8; ++c) {
      const unsigned t0 = x0[c >> 1], t1 = x1[c >> 1];
      f32x2 v = (c & 1) ? f32x2{asf(t0 & 0xffff0000u), asf(t1 & 0xffff0000u)} : f32x2{asf(t0 << 16), asf(t1 << 16)};
      v -= m;
      dv[c] = v;
      qq += v * v;
    }
    const f32x2 rs = both(__builtin_amdgcn_rsqf(reduce2(qq[0], qq[1]) * (1.0f / D) + 1e-5f));
    LAS unsigned char* lt = (LAS unsigned char*)lds + L_LN;
#pragma unroll
    for (int c = 0; c < 8; ++c) {
      const f32x2 v = dv[c] * rs;
      *(LAS unsigned*)(lt + (lane >> 3) * 1024 + c * 128 + (lane & 7) * 16 + w * 4) = pk_bf16(v[0], v[1]);
    }
    const i32x4 srdh = make_srd(wsd + WS_H + w * 2 * H_TILE + (b >> 1) * 2048 + (b & 1) * 512, 2 * H_TILE);
    asm volatile(
        "s_waitcnt lgkmcnt(0)\n"
        "s_barrier\n"
        "ds_read_b128 v[80:83], %[vlt] offset:0\n"
        "ds_read_b128 v[84:87], %[vlt] offset:1024\n"
        "s_mov_b32 %[tmp], 262144\n"
        "s_waitcnt lgkmcnt(1)\n"
        "buffer_store_dwordx4 v[80:83], %[vho], %[srdh], 0 offen sc1\n"
        "s_waitcnt lgkmcnt(0)\n"
        "buffer_store_dwordx4 v[84:87], %[vho], %[srdh], %[tmp] offen sc1\n"
        : [tmp] "=&s"(tmp)
        : [vlt] "v"(ldsb + L_LN + w * 2048 + vl16), [vho] "v"(vl16 + (lane >> 5) * 512), [srdh] "s"(srdh)
        : "memory", C10("v", 8));
  }
  const unsigned vfo = lane ? (unsigned)WS_CTRL : WS_FL + b * 128u + w * 4u;
  STAMP(2);
  const int hh = lane >> 5, tq = (lane & 15) >> 2;
  const unsigned trc = ring + 32 * ((lane >> 4) & 1) + 8 * (lane & 3);
  const int st = 1 ? xc * 4 + (j >> 3) : q * 8 + (j & 7), dt2 = 1 ? (j & 7) : half * 4 + (j >> 3);
  const unsigned vx0 = (lane >> 3) * P2 + ((lane & 7) ^ (((lane >> 4) & 1) << 2)) * 16;
  const unsigned vx1 = vx0 + 8 * P2 - 1024, vx2 = vx0 + 16 * P2 - 2048, vx3 = vx0 + 24 * P2 - 3072;
  const int rt2 = (((j & 7) * 11) >> 3) + (j >> 3) * 0, rot2 = rt2 >= 11 ? rt2 - 11 : rt2;
  const unsigned soxs = (unsigned)w * (352 * P2), soxe = soxs + 352 * P2;
  unsigned sox = soxs + rot2 * (32 * P2);
  const unsigned tq0 = trc + (4 * hh + tq) * 128 + 64 * ((w >> 1) ^ (tq >> 1)), tq1 = tq0 ^ 64;
  const unsigned ringx = ldsb + L_KEEP + w * 8192, tqx0 = tq0 - ring + ringx, tqx1 = tqx0 ^ 64;
  const unsigned ringy = ldsb + L_XCH + w * 4096, tqy = tq0 - ring + ringy;
  const i32x4 srdx = make_srd(ka[3] + st * 128, (unsigned)(F * P2 - st * 128));
  const i32x4 srd2 = make_srd(ka[4] + st * 128 + (w >> 1) * 64, 64);
  const unsigned vb2 = (lane & 31) * 2;
  unsigned b2v;
#define W2_IN [vtag] "v"(tag), [vfo] "v"(vfo), [srdc] "s"(srdc), [ring] "s"(ring), [vx0] "v"(vx0), [vx1] "v"(vx1), [vx2] "v"(vx2), [vx3] "v"(vx3), [srdx] "s"(srdx), [soxe] "s"(soxe), \
              [soxs] "s"(soxs), [tq0] "v"(tq0), [tq1] "v"(tq1), [ringx] "s"(ringx), [ringy] "s"(ringy), [tqx0] "v"(tqx0), [tqx1] "v"(tqx1)

  unsigned va = vl16 + (w & 1) * 1024, vb = va ^ 1024, tqy0 = tqy, tqy1 = tqy ^ 64;
  unsigned xr2 = ldsb + L_XCH2 + w * 12288 + vl16, xw1 = ldsb + L_XCH2 + (w ^ 1) * 12288 + vl16,
           xw2 = ldsb + L_XCH2 + (w ^ 2) * 12288 + vl16, xw3 = ldsb + L_XCH2 + (w ^ 3) * 12288 + vl16;
  unsigned ow = ldsb + L_OUT + ((w >> 1) * 32 + (lane & 31)) * 144 + (32 * hh + 4 * (w & 1)) * 2;
  unsigned orl = ldsb + L_OUT + (w * 16 + (lane >> 3)) * 144 + (lane & 7) * 16, og = (lane >> 3) * 1024 + (lane & 7) * 16;
  asm volatile("" : "+v"(va), "+v"(vb), "+v"(tqy0), "+v"(tqy1), "+v"(xr2), "+v"(xw1), "+v"(xw2), "+v"(xw3), "+v"(ow), "+v"(orl), "+v"(og));
  i32x4 srdg = make_srd(wsd + goff, gown), srdG = make_srd(wsd + WS_G + dt2 * G_TILE, G_TILE);
  i32x4 srdo = make_srd(ka[5] + (st * 64 + w * 16) * 1024 + dt2 * 128, 15 * 1024 + 128);
  unsigned sgs = (unsigned)w * (11 * 4096), sge = sgs + 11 * 4096, so0 = sgs + rot2 * 4096;
  asm volatile("" : "+s"(srdg), "+s"(srdG), "+s"(srdo), "+s"(sgs), "+s"(sge), "+s"(so0));

  if (ft < 15) {
    const int n0 = w == 3 ? 0 : w, n1 = n0 == 2 ? 0 : n0 + 1, n2 = n0 == 0 ? 2 : n0 - 1;
    const unsigned pr = (tq + pb) & 1, tf = trc + (8 * hh + tq) * 128, th = trc + 2048 + (4 * hh + (tq >> 1) + 2 * (tq & 1)) * 64;
    auto tra = [&](int n) { return n == (pr ? 0 : 2) ? th : tf + 64 * ((n - pr) ^ (tq >> 1)); };
    const unsigned tr0 = tra(n0), tr1 = tra(n1), tr2 = tra(n2);
    const unsigned xa0 = ldsb + L_XCH + 8192 + vl16, xa1 = ldsb + L_XCH + n1 * 12288 + (w == 3 ? 8192 : 0) + vl16,
                   xa2 = ldsb + L_XCH + n2 * 12288 + (w == 3 ? 8192 : 4096) + vl16, xr = ldsb + L_XCH + n0 * 12288 + vl16;
    const unsigned vhk = vl16 + half * 1024, vhs = vhk ^ 1024;
    const unsigned soas = (unsigned)(half * 4 + w) * 32768u, soae = soas + 32768u;
    unsigned soa = soas + rot * 2048;
    const unsigned vfl = WS_FL + ((half * 4 + w) * 32 + (lane & 31)) * 128;
    const unsigned own = w < 3 ? 4096u : 0u;
    const i32x4 srdb = make_srd(ka[2] + (f0 + n0 * 32) * 2, own >> 6);
    const i32x4 srda = make_srd(wsd + WS_H + dt * H_TILE, H_TILE);
    const i32x4 srds = make_srd(wsd + WS_X + (((ft * 8 + dt) * 2 + half) * 3 + n0) * 4096, own);
    const i32x4 srdr = make_srd(wsd + WS_X + (((ft * 8 + dt) * 2 + (half ^ 1)) * 3 + n0) * 4096, own);
    f32x16 acc;
    asm volatile(
        "s_cmp_lg_u32 %[ph], 0\n"
        "s_cbranch_scc1 .Lf0%=\n"
        "s_add_u32 m0, %[ring], 6144\n"
        "s_add_u32 %[tmp], %[sow], 44032\n"
        "buffer_load_dwordx4 %[vw0], %[srdw], %[sow] offen lds\n"
        "buffer_load_dwordx4 %[vw1], %[srdw], %[sow] offen offset:1024 lds\n"
        "buffer_load_dwordx4 %[vw2], %[srdw], %[sow] offen offset:2048 lds\n"
        "s_cmp_eq_u32 %[tmp], %[sowe]\n"
        "s_cselect_b32 %[sow], %[sows], %[tmp]\n"
        "s_waitcnt vmcnt(8)\n"
        "ds_read_b64_tr_b16 a[0:1], %[tr0] offset:0\n"
        "ds_read_b64_tr_b16 a[2:3], %[tr0] offset:512\n"
        "ds_read_b64_tr_b16 a[4:5], %[tr1] offset:0\n"
        "ds_read_b64_tr_b16 a[6:7], %[tr1] offset:512\n"
        "ds_read_b64_tr_b16 a[8:9], %[tr2] offset:0\n"
        "ds_read_b64_tr_b16 a[10:11], %[tr2] offset:512\n"
        "s_add_u32 m0, %[ring], 9216\n"
        "s_add_u32 %[tmp], %[sow], 44032\n"
        "buffer_load_dwordx4 %[vw0], %[srdw], %[sow] offen lds\n"
        "buffer_load_dwordx4 %[vw1], %[srdw], %[sow] offen offset:1024 lds\n"
        "buffer_load_dwordx4 %[vw2], %[srdw], %[sow] offen offset:2048 lds\n"
        "s_cmp_eq_u32 %[tmp], %[sowe]\n"
        "s_cselect_b32 %[sow], %[sows], %[tmp]\n"
        "s_waitcnt vmcnt(8)\n"
        "ds_read_b64_tr_b16 a[12:13], %[tr0] offset:3072\n"
        "ds_read_b64_tr_b16 a[14:15], %[tr0] offset:3584\n"
        "ds_read_b64_tr_b16 a[16:17], %[tr1] offset:3072\n"
        "ds_read_b64_tr_b16 a[18:19], %[tr1] offset:3584\n"
        "ds_read_b64_tr_b16 a[20:21], %[tr2] offset:3072\n"
        "ds_read_b64_tr_b16 a[22:23], %[tr2] offset:3584\n"
        "s_waitcnt vmcnt(6)\n"
        "buffer_store_dword %[vtag], %[vfo], %[srdc], 0 offen sc1\n"
        "s_add_u32 m0, %[ring], 12288\n"
        "s_add_u32 %[tmp], %[sow], 44032\n"
        "buffer_load_dwordx4 %[vw0], %[srdw], %[sow] offen lds\n"
        "buffer_load_dwordx4 %[vw1], %[srdw], %[sow] offen offset:1024 lds\n"
        "buffer_load_dwordx4 %[vw2], %[srdw], %[sow] offen offset:2048 lds\n"
        "s_cmp_eq_u32 %[tmp], %[sowe]\n"
        "s_cselect_b32 %[sow], %[sows], %[tmp]\n"
        "buffer_load_dwordx2 v[242:243], %[vbias], %[srdb], 0 offen offset:0\n"
        "buffer_load_dwordx2 v[244:245], %[vbias], %[srdb], 0 offen offset:16\n"
        "buffer_load_dwordx2 v[246:247], %[vbias], %[srdb], 0 offen offset:32\n"
        "buffer_load_dwordx2 v[248:249], %[vbias], %[srdb], 0 offen offset:48\n"
        "buffer_load_ushort %[b2v], %[vb2], %[srd2], 0 offen\n"
        "s_waitcnt vmcnt(12) lgkmcnt(7)\n"
        "ds_read_b64_tr_b16 a[24:25], %[tr0] offset:6144\n"
        "ds_read_b64_tr_b16 a[26:27], %[tr0] offset:6656\n"
        "ds_read_b64_tr_b16 a[28:29], %[tr1] offset:6144\n"
        "ds_read_b64_tr_b16 a[30:31], %[tr1] offset:6656\n"
        "ds_read_b64_tr_b16 a[32:33], %[tr2] offset:6144\n"
        "ds_read_b64_tr_b16 a[34:35], %[tr2] offset:6656\n"
        "s_add_u32 m0, %[ringx], 0\n"
        "s_add_u32 %[tmp], %[sox], 131072\n"
        "buffer_load_dwordx4 %[vx0], %[srdx], %[sox] offen lds\n"
        "buffer_load_dwordx4 %[vx1], %[srdx], %[sox] offen offset:1024 lds\n"
        "buffer_load_dwordx4 %[vx2], %[srdx], %[sox] offen offset:2048 lds\n"
        "buffer_load_dwordx4 %[vx3], %[srdx], %[sox] offen offset:3072 lds\n"
        "s_cmp_eq_u32 %[tmp], %[soxe]\n"
        "s_cselect_b32 %[sox], %[soxs], %[tmp]\n"
        "s_waitcnt vmcnt(13) lgkmcnt(7)\n"
        "ds_read_b64_tr_b16 a[36:37], %[tr0] offset:9216\n"
        "ds_read_b64_tr_b16 a[38:39], %[tr0] offset:9728\n"
        "ds_read_b64_tr_b16 a[40:41], %[tr1] offset:9216\n"
        "ds_read_b64_tr_b16 a[42:43], %[tr1] offset:9728\n"
        "ds_read_b64_tr_b16 a[44:45], %[tr2] offset:9216\n"
        "ds_read_b64_tr_b16 a[46:47], %[tr2] offset:9728\n"
        "s_add_u32 m0, %[ringx], 4096\n"
        "s_add_u32 %[tmp], %[sox], 131072\n"
        "buffer_load_dwordx4 %[vx0], %[srdx], %[sox] offen lds\n"
        "buffer_load_dwordx4 %[vx1], %[srdx], %[sox] offen offset:1024 lds\n"
        "buffer_load_dwordx4 %[vx2], %[srdx], %[sox] offen offset:2048 lds\n"
        "buffer_load_dwordx4 %[vx3], %[srdx], %[sox] offen offset:3072 lds\n"
        "s_cmp_eq_u32 %[tmp], %[soxe]\n"
        "s_cselect_b32 %[sox], %[soxs], %[tmp]\n"
        "s_waitcnt vmcnt(13) lgkmcnt(7)\n"
        "ds_read_b64_tr_b16 a[48:49], %[tr0] offset:12288\n"
        "ds_read_b64_tr_b16 a[50:51], %[tr0] offset:12800\n"
        "ds_read_b64_tr_b16 a[52:53], %[tr1] offset:12288\n"
        "ds_read_b64_tr_b16 a[54:55], %[tr1] offset:12800\n"
        "ds_read_b64_tr_b16 a[56:57], %[tr2] offset:12288\n"
        "ds_read_b64_tr_b16 a[58:59], %[tr2] offset:12800\n"
        "s_add_u32 m0, %[ring], 15360\n"
        "s_add_u32 %[tmp], %[sow], 44032\n"
        "buffer_load_dwordx4 %[vw0], %[srdw], %[sow] offen lds\n"
        "buffer_load_dwordx4 %[vw1], %[srdw], %[sow] offen offset:1024 lds\n"
        "buffer_load_dwordx4 %[vw2], %[srdw], %[sow] offen offset:2048 lds\n"
        "s_cmp_eq_u32 %[tmp], %[sowe]\n"
        "s_cselect_b32 %[sow], %[sows], %[tmp]\n"
        "s_waitcnt vmcnt(7) lgkmcnt(7)\n"
        "ds_read_b64_tr_b16 a[192:193], %[tqx0] offset:0\n"
        "ds_read_b64_tr_b16 a[194:195], %[tqx0] offset:1024\n"
        "ds_read_b64_tr_b16 a[196:197], %[tqx1] offset:0\n"
        "ds_read_b64_tr_b16 a[198:199], %[tqx1] offset:1024\n"
        "ds_read_b64_tr_b16 a[200:201], %[tqx0] offset:2048\n"
        "ds_read_b64_tr_b16 a[202:203], %[tqx0] offset:3072\n"
        "ds_read_b64_tr_b16 a[204:205], %[tqx1] offset:2048\n"
        "ds_read_b64_tr_b16 a[206:207], %[tqx1] offset:3072\n"
        "s_add_u32 m0, %[ring], 0\n"
        "s_add_u32 %[tmp], %[sow], 44032\n"
        "buffer_load_dwordx4 %[vw0], %[srdw], %[sow] offen lds\n"
        "buffer_load_dwordx4 %[vw1], %[srdw], %[sow] offen offset:1024 lds\n"
        "buffer_load_dwordx4 %[vw2], %[srdw], %[sow] offen offset:2048 lds\n"
        "s_cmp_eq_u32 %[tmp], %[sowe]\n"
        "s_cselect_b32 %[sow], %[sows], %[tmp]\n"
        "s_waitcnt vmcnt(6) lgkmcnt(7)\n"
        "ds_read_b64_tr_b16 a[208:209], %[tqx0] offset:4096\n"
        "ds_read_b64_tr_b16 a[210:211], %[tqx0] offset:5120\n"
        "ds_read_b64_tr_b16 a[212:213], %[tqx1] offset:4096\n"
        "ds_read_b64_tr_b16 a[214:215], %[tqx1] offset:5120\n"
        "ds_read_b64_tr_b16 a[216:217], %[tqx0] offset:6144\n"
        "ds_read_b64_tr_b16 a[218:219], %[tqx0] offset:7168\n"
        "ds_read_b64_tr_b16 a[220:221], %[tqx1] offset:6144\n"
        "ds_read_b64_tr_b16 a[222:223], %[tqx1] offset:7168\n"
        "v_lshlrev_b32 v64, 16, v242\n"
        "v_and_b32 v65, 0xffff0000, v242\n"
        "v_lshlrev_b32 v66, 16, v243\n"
        "v_and_b32 v67, 0xffff0000, v243\n"
        "v_lshlrev_b32 v68, 16, v244\n"
        "v_and_b32 v69, 0xffff0000, v244\n"
        "v_lshlrev_b32 v70, 16, v245\n"
        "v_and_b32 v71, 0xffff0000, v245\n"
        "v_lshlrev_b32 v72, 16, v246\n"
        "v_and_b32 v73, 0xffff0000, v246\n"
        "v_lshlrev_b32 v74, 16, v247\n"
        "v_and_b32 v75, 0xffff0000, v247\n"
        "v_lshlrev_b32 v76, 16, v248\n"
        "v_and_b32 v77, 0xffff0000, v248\n"
        "v_lshlrev_b32 v78, 16, v249\n"
        "v_and_b32 v79, 0xffff0000, v249\n"
        "v_mov_b32 v250, 0xffffff00\n"
        "s_add_u32 m0, %[ring], 3072\n"
        "s_add_u32 %[tmp], %[sow], 44032\n"
        "buffer_load_dwordx4 %[vw0], %[srdw], %[sow] offen lds\n"
        "buffer_load_dwordx4 %[vw1], %[srdw], %[sow] offen offset:1024 lds\n"
        "buffer_load_dwordx4 %[vw2], %[srdw], %[sow] offen offset:2048 lds\n"
        "s_cmp_eq_u32 %[tmp], %[sowe]\n"
        "s_cselect_b32 %[sow], %[sows], %[tmp]\n"
        "s_waitcnt vmcnt(6) lgkmcnt(7)\n"
        "ds_read_b64_tr_b16 a[60:61], %[tr0] offset:15360\n"
        "ds_read_b64_tr_b16 a[62:63], %[tr0] offset:15872\n"
        "ds_read_b64_tr_b16 a[64:65], %[tr1] offset:15360\n"
        "ds_read_b64_tr_b16 a[66:67], %[tr1] offset:15872\n"
        "ds_read_b64_tr_b16 a[68:69], %[tr2] offset:15360\n"
        "ds_read_b64_tr_b16 a[70:71], %[tr2] offset:15872\n"
        STAMP_ASM(3)
        "s_add_u32 m0, %[ring], 6144\n"
        "s_add_u32 %[tmp], %[sow], 44032\n"
        "buffer_load_dwordx4 %[vw0], %[srdw], %[sow] offen lds\n"
        "buffer_load_dwordx4 %[vw1], %[srdw], %[sow] offen offset:1024 lds\n"
        "buffer_load_dwordx4 %[vw2], %[srdw], %[sow] offen offset:2048 lds\n"
        "s_cmp_eq_u32 %[tmp], %[sowe]\n"
        "s_cselect_b32 %[sow], %[sows], %[tmp]\n"
        "s_waitcnt vmcnt(6) lgkmcnt(7)\n"
        "ds_read_b64_tr_b16 a[72:73], %[tr0] offset:0\n"
        "ds_read_b64_tr_b16 a[74:75], %[tr0] offset:512\n"
        "ds_read_b64_tr_b16 a[76:77], %[tr1] offset:0\n"
        "ds_read_b64_tr_b16 a[78:79], %[tr1] offset:512\n"
        "ds_read_b64_tr_b16 a[80:81], %[tr2] offset:0\n"
        "ds_read_b64_tr_b16 a[82:83], %[tr2] offset:512\n"
        "s_add_u32 m0, %[ring], 9216\n"
        "s_add_u32 %[tmp], %[sow], 44032\n"
        "buffer_load_dwordx4 %[vw0], %[srdw], %[sow] offen lds\n"
        "buffer_load_dwordx4 %[vw1], %[srdw], %[sow] offen offset:1024 lds\n"
        "buffer_load_dwordx4 %[vw2], %[srdw], %[sow] offen offset:2048 lds\n"
        "s_cmp_eq_u32 %[tmp], %[sowe]\n"
        "s_cselect_b32 %[sow], %[sows], %[tmp]\n"
        "s_waitcnt vmcnt(6) lgkmcnt(7)\n"
        "ds_read_b64_tr_b16 a[84:85], %[tr0] offset:3072\n"
        "ds_read_b64_tr_b16 a[86:87], %[tr0] offset:3584\n"
        "ds_read_b64_tr_b16 a[88:89], %[tr1] offset:3072\n"
        "ds_read_b64_tr_b16 a[90:91], %[tr1] offset:3584\n"
        "ds_read_b64_tr_b16 a[92:93], %[tr2] offset:3072\n"
        "ds_read_b64_tr_b16 a[94:95], %[tr2] offset:3584\n"
        "s_add_u32 m0, %[ring], 12288\n"
        "s_add_u32 %[tmp], %[sow], 44032\n"
        "buffer_load_dwordx4 %[vw0], %[srdw], %[sow] offen lds\n"
        "buffer_load_dwordx4 %[vw1], %[srdw], %[sow] offen offset:1024 lds\n"
        "buffer_load_dwordx4 %[vw2], %[srdw], %[sow] offen offset:2048 lds\n"
        "s_cmp_eq_u32 %[tmp], %[sowe]\n"
        "s_cselect_b32 %[sow], %[sows], %[tmp]\n"
        "s_waitcnt vmcnt(6) lgkmcnt(7)\n"
        "ds_read_b64_tr_b16 a[96:97], %[tr0] offset:6144\n"
        "ds_read_b64_tr_b16 a[98:99], %[tr0] offset:6656\n"
        "ds_read_b64_tr_b16 a[100:101], %[tr1] offset:6144\n"
        "ds_read_b64_tr_b16 a[102:103], %[tr1] offset:6656\n"
        "ds_read_b64_tr_b16 a[104:105], %[tr2] offset:6144\n"
        "ds_read_b64_tr_b16 a[106:107], %[tr2] offset:6656\n"
        "s_add_u32 m0, %[ring], 15360\n"
        "s_add_u32 %[tmp], %[sow], 44032\n"
        "buffer_load_dwordx4 %[vw0], %[srdw], %[sow] offen lds\n"
        "buffer_load_dwordx4 %[vw1], %[srdw], %[sow] offen offset:1024 lds\n"
        "buffer_load_dwordx4 %[vw2], %[srdw], %[sow] offen offset:2048 lds\n"
        "s_cmp_eq_u32 %[tmp], %[sowe]\n"
        "s_cselect_b32 %[sow], %[sows], %[tmp]\n"
        "s_waitcnt vmcnt(6) lgkmcnt(7)\n"
        "ds_read_b64_tr_b16 a[108:109], %[tr0] offset:9216\n"
        "ds_read_b64_tr_b16 a[110:111], %[tr0] offset:9728\n"
        "ds_read_b64_tr_b16 a[112:113], %[tr1] offset:9216\n"
        "ds_read_b64_tr_b16 a[114:115], %[tr1] offset:9728\n"
        "ds_read_b64_tr_b16 a[116:117], %[tr2] offset:9216\n"
        "ds_read_b64_tr_b16 a[118:119], %[tr2] offset:9728\n"
        "s_add_u32 m0, %[ring], 0\n"
        "s_add_u32 %[tmp], %[sow], 44032\n"
        "buffer_load_dwordx4 %[vw0], %[srdw], %[sow] offen lds\n"
        "buffer_load_dwordx4 %[vw1], %[srdw], %[sow] offen offset:1024 lds\n"
        "buffer_load_dwordx4 %[vw2], %[srdw], %[sow] offen offset:2048 lds\n"
        "s_cmp_eq_u32 %[tmp], %[sowe]\n"
        "s_cselect_b32 %[sow], %[sows], %[tmp]\n"
        "s_waitcnt vmcnt(6) lgkmcnt(7)\n"
        "ds_read_b64_tr_b16 a[120:121], %[tr0] offset:12288\n"
        "ds_read_b64_tr_b16 a[122:123], %[tr0] offset:12800\n"
        "ds_read_b64_tr_b16 a[124:125], %[tr1] offset:12288\n"
        "ds_read_b64_tr_b16 a[126:127], %[tr1] offset:12800\n"
        "ds_read_b64_tr_b16 a[128:129], %[tr2] offset:12288\n"
        "ds_read_b64_tr_b16 a[130:131], %[tr2] offset:12800\n"
        "s_add_u32 m0, %[ring], 3072\n"
        "s_add_u32 %[tmp], %[sow], 44032\n"
        "buffer_load_dwordx4 %[vw0], %[srdw], %[sow] offen lds\n"
        "buffer_load_dwordx4 %[vw1], %[srdw], %[sow] offen offset:1024 lds\n"
        "buffer_load_dwordx4 %[vw2], %[srdw], %[sow] offen offset:2048 lds\n"
        "s_cmp_eq_u32 %[tmp], %[sowe]\n"
        "s_cselect_b32 %[sow], %[sows], %[tmp]\n"
        "buffer_load_dwordx4 v[236:239], %[vfl], %[srdc], 0 offen sc1\n"
        "s_waitcnt vmcnt(7) lgkmcnt(7)\n"
        "ds_read_b64_tr_b16 a[132:133], %[tr0] offset:15360\n"
        "ds_read_b64_tr_b16 a[134:135], %[tr0] offset:15872\n"
        "ds_read_b64_tr_b16 a[136:137], %[tr1] offset:15360\n"
        "ds_read_b64_tr_b16 a[138:139], %[tr1] offset:15872\n"
        "ds_read_b64_tr_b16 a[140:141], %[tr2] offset:15360\n"
        "ds_read_b64_tr_b16 a[142:143], %[tr2] offset:15872\n"
        "s_add_u32 m0, %[ring], 6144\n"
        "s_add_u32 %[tmp], %[sow], 44032\n"
        "buffer_load_dwordx4 %[vw0], %[srdw], %[sow] offen lds\n"
        "buffer_load_dwordx4 %[vw1], %[srdw], %[sow] offen offset:1024 lds\n"
        "buffer_load_dwordx4 %[vw2], %[srdw], %[sow] offen offset:2048 lds\n"
        "s_cmp_eq_u32 %[tmp], %[sowe]\n"
        "s_cselect_b32 %[sow], %[sows], %[tmp]\n"
        "s_waitcnt vmcnt(7) lgkmcnt(7)\n"
        "ds_read_b64_tr_b16 a[144:145], %[tr0] offset:0\n"
        "ds_read_b64_tr_b16 a[146:147], %[tr0] offset:512\n"
        "ds_read_b64_tr_b16 a[148:149], %[tr1] offset:0\n"
        "ds_read_b64_tr_b16 a[150:151], %[tr1] offset:512\n"
        "ds_read_b64_tr_b16 a[152:153], %[tr2] offset:0\n"
        "ds_read_b64_tr_b16 a[154:155], %[tr2] offset:512\n"
        "s_add_u32 m0, %[ring], 9216\n"
        "s_add_u32 %[tmp], %[sow], 44032\n"
        "buffer_load_dwordx4 %[vw0], %[srdw], %[sow] offen lds\n"
        "buffer_load_dwordx4 %[vw1], %[srdw], %[sow] offen offset:1024 lds\n"
        "buffer_load_dwordx4 %[vw2], %[srdw], %[sow] offen offset:2048 lds\n"
        "s_cmp_eq_u32 %[tmp], %[sowe]\n"
        "s_cselect_b32 %[sow], %[sows], %[tmp]\n"
        "s_waitcnt vmcnt(7) lgkmcnt(7)\n"
        "ds_read_b64_tr_b16 a[156:157], %[tr0] offset:3072\n"
        "ds_read_b64_tr_b16 a[158:159], %[tr0] offset:3584\n"
        "ds_read_b64_tr_b16 a[160:161], %[tr1] offset:3072\n"
        "ds_read_b64_tr_b16 a[162:163], %[tr1] offset:3584\n"
        "ds_read_b64_tr_b16 a[164:165], %[tr2] offset:3072\n"
        "ds_read_b64_tr_b16 a[166:167], %[tr2] offset:3584\n"
        "s_waitcnt vmcnt(6) lgkmcnt(0)\n"
        "s_branch .Lf1%=\n"
        ".Lf0%=:\n"
        "s_add_u32 m0, %[ring], 6144\n"
        "s_add_u32 %[tmp], %[sow], 44032\n"
        "buffer_load_dwordx4 %[vw0], %[srdw], %[sow] offen lds\n"
        "buffer_load_dwordx4 %[vw1], %[srdw], %[sow] offen offset:1024 lds\n"
        "buffer_load_dwordx4 %[vw2], %[srdw], %[sow] offen offset:2048 lds\n"
        "s_cmp_eq_u32 %[tmp], %[sowe]\n"
        "s_cselect_b32 %[sow], %[sows], %[tmp]\n"
        "s_waitcnt vmcnt(8)\n"
        "ds_read_b64_tr_b16 a[0:1], %[tr0] offset:0\n"
        "ds_read_b64_tr_b16 a[2:3], %[tr0] offset:512\n"
        "ds_read_b64_tr_b16 a[4:5], %[tr1] offset:0\n"
        "ds_read_b64_tr_b16 a[6:7], %[tr1] offset:512\n"
        "ds_read_b64_tr_b16 a[8:9], %[tr2] offset:0\n"
        "ds_read_b64_tr_b16 a[10:11], %[tr2] offset:512\n"
        "s_add_u32 m0, %[ring], 9216\n"
        "s_add_u32 %[tmp], %[sow], 44032\n"
        "buffer_load_dwordx4 %[vw0], %[srdw], %[sow] offen lds\n"
        "buffer_load_dwordx4 %[vw1], %[srdw], %[sow] offen offset:1024 lds\n"
        "buffer_load_dwordx4 %[vw2], %[srdw], %[sow] offen offset:2048 lds\n"
        "s_cmp_eq_u32 %[tmp], %[sowe]\n"
        "s_cselect_b32 %[sow], %[sows], %[tmp]\n"
        "s_waitcnt vmcnt(8)\n"
        "ds_read_b64_tr_b16 a[12:13], %[tr0] offset:3072\n"
        "ds_read_b64_tr_b16 a[14:15], %[tr0] offset:3584\n"
        "ds_read_b64_tr_b16 a[16:17], %[tr1] offset:3072\n"
        "ds_read_b64_tr_b16 a[18:19], %[tr1] offset:3584\n"
        "ds_read_b64_tr_b16 a[20:21], %[tr2] offset:3072\n"
        "ds_read_b64_tr_b16 a[22:23], %[tr2] offset:3584\n"
        "s_waitcnt vmcnt(6)\n"
        "buffer_store_dword %[vtag], %[vfo], %[srdc], 0 offen sc1\n"
        "s_add_u32 m0, %[ring], 12288\n"
        "s_add_u32 %[tmp], %[sow], 44032\n"
        "buffer_load_dwordx4 %[vw0], %[srdw], %[sow] offen lds\n"
        "buffer_load_dwordx4 %[vw1], %[srdw], %[sow] offen offset:1024 lds\n"
        "buffer_load_dwordx4 %[vw2], %[srdw], %[sow] offen offset:2048 lds\n"
        "s_cmp_eq_u32 %[tmp], %[sowe]\n"
        "s_cselect_b32 %[sow], %[sows], %[tmp]\n"
        "buffer_load_dwordx2 v[242:243], %[vbias], %[srdb], 0 offen offset:0\n"
        "buffer_load_dwordx2 v[244:245], %[vbias], %[srdb], 0 offen offset:16\n"
        "buffer_load_dwordx2 v[246:247], %[vbias], %[srdb], 0 offen offset:32\n"
        "buffer_load_dwordx2 v[248:249], %[vbias], %[srdb], 0 offen offset:48\n"
        "buffer_load_ushort %[b2v], %[vb2], %[srd2], 0 offen\n"
        "s_waitcnt vmcnt(12) lgkmcnt(7)\n"
        "ds_read_b64_tr_b16 a[24:25], %[tr0] offset:6144\n"
        "ds_read_b64_tr_b16 a[26:27], %[tr0] offset:6656\n"
        "ds_read_b64_tr_b16 a[28:29], %[tr1] offset:6144\n"
        "ds_read_b64_tr_b16 a[30:31], %[tr1] offset:6656\n"
        "ds_read_b64_tr_b16 a[32:33], %[tr2] offset:6144\n"
        "ds_read_b64_tr_b16 a[34:35], %[tr2] offset:6656\n"
        "s_add_u32 m0, %[ring], 15360\n"
        "s_add_u32 %[tmp], %[sow], 44032\n"
        "buffer_load_dwordx4 %[vw0], %[srdw], %[sow] offen lds\n"
        "buffer_load_dwordx4 %[vw1], %[srdw], %[sow] offen offset:1024 lds\n"
        "buffer_load_dwordx4 %[vw2], %[srdw], %[sow] offen offset:2048 lds\n"
        "s_cmp_eq_u32 %[tmp], %[sowe]\n"
        "s_cselect_b32 %[sow], %[sows], %[tmp]\n"
        "s_waitcnt vmcnt(12) lgkmcnt(7)\n"
        "ds_read_b64_tr_b16 a[36:37], %[tr0] offset:9216\n"
        "ds_read_b64_tr_b16 a[38:39], %[tr0] offset:9728\n"
        "ds_read_b64_tr_b16 a[40:41], %[tr1] offset:9216\n"
        "ds_read_b64_tr_b16 a[42:43], %[tr1] offset:9728\n"
        "ds_read_b64_tr_b16 a[44:45], %[tr2] offset:9216\n"
        "ds_read_b64_tr_b16 a[46:47], %[tr2] offset:9728\n"
        "s_add_u32 m0, %[ring], 0\n"
        "s_add_u32 %[tmp], %[sow], 44032\n"
        "buffer_load_dwordx4 %[vw0], %[srdw], %[sow] offen lds\n"
        "buffer_load_dwordx4 %[vw1], %[srdw], %[sow] offen offset:1024 lds\n"
        "buffer_load_dwordx4 %[vw2], %[srdw], %[sow] offen offset:2048 lds\n"
        "s_cmp_eq_u32 %[tmp], %[sowe]\n"
        "s_cselect_b32 %[sow], %[sows], %[tmp]\n"
        "s_waitcnt vmcnt(11) lgkmcnt(7)\n"
        "ds_read_b64_tr_b16 a[48:49], %[tr0] offset:12288\n"
        "ds_read_b64_tr_b16 a[50:51], %[tr0] offset:12800\n"
        "ds_read_b64_tr_b16 a[52:53], %[tr1] offset:12288\n"
        "ds_read_b64_tr_b16 a[54:55], %[tr1] offset:12800\n"
        "ds_read_b64_tr_b16 a[56:57], %[tr2] offset:12288\n"
        "ds_read_b64_tr_b16 a[58:59], %[tr2] offset:12800\n"
        "s_add_u32 m0, %[ring], 3072\n"
        "s_add_u32 %[tmp], %[sow], 44032\n"
        "buffer_load_dwordx4 %[vw0], %[srdw], %[sow] offen lds\n"
        "buffer_load_dwordx4 %[vw1], %[srdw], %[sow] offen offset:1024 lds\n"
        "buffer_load_dwordx4 %[vw2], %[srdw], %[sow] offen offset:2048 lds\n"
        "s_cmp_eq_u32 %[tmp], %[sowe]\n"
        "s_cselect_b32 %[sow], %[sows], %[tmp]\n"
        "s_waitcnt vmcnt(6) lgkmcnt(7)\n"
        "ds_read_b64_tr_b16 a[60:61], %[tr0] offset:15360\n"
        "ds_read_b64_tr_b16 a[62:63], %[tr0] offset:15872\n"
        "ds_read_b64_tr_b16 a[64:65], %[tr1] offset:15360\n"
        "ds_read_b64_tr_b16 a[66:67], %[tr1] offset:15872\n"
        "ds_read_b64_tr_b16 a[68:69], %[tr2] offset:15360\n"
        "ds_read_b64_tr_b16 a[70:71], %[tr2] offset:15872\n"
        "s_add_u32 m0, %[ring], 6144\n"
        "s_add_u32 %[tmp], %[sow], 44032\n"
        "buffer_load_dwordx4 %[vw0], %[srdw], %[sow] offen lds\n"
        "buffer_load_dwordx4 %[vw1], %[srdw], %[sow] offen offset:1024 lds\n"
        "buffer_load_dwordx4 %[vw2], %[srdw], %[sow] offen offset:2048 lds\n"
        "s_cmp_eq_u32 %[tmp], %[sowe]\n"
        "s_cselect_b32 %[sow], %[sows], %[tmp]\n"
        "s_waitcnt vmcnt(6) lgkmcnt(7)\n"
        "ds_read_b64_tr_b16 a[72:73], %[tr0] offset:0\n"
        "ds_read_b64_tr_b16 a[74:75], %[tr0] offset:512\n"
        "ds_read_b64_tr_b16 a[76:77], %[tr1] offset:0\n"
        "ds_read_b64_tr_b16 a[78:79], %[tr1] offset:512\n"
        "ds_read_b64_tr_b16 a[80:81], %[tr2] offset:0\n"
        "ds_read_b64_tr_b16 a[82:83], %[tr2] offset:512\n"
        "v_lshlrev_b32 v64, 16, v242\n"
        "v_and_b32 v65, 0xffff0000, v242\n"
        "v_lshlrev_b32 v66, 16, v243\n"
        "v_and_b32 v67, 0xffff0000, v243\n"
        "v_lshlrev_b32 v68, 16, v244\n"
        "v_and_b32 v69, 0xffff0000, v244\n"
        "v_lshlrev_b32 v70, 16, v245\n"
        "v_and_b32 v71, 0xffff0000, v245\n"
        "v_lshlrev_b32 v72, 16, v246\n"
        "v_and_b32 v73, 0xffff0000, v246\n"
        "v_lshlrev_b32 v74, 16, v247\n"
        "v_and_b32 v75, 0xffff0000, v247\n"
        "v_lshlrev_b32 v76, 16, v248\n"
        "v_and_b32 v77, 0xffff0000, v248\n"
        "v_lshlrev_b32 v78, 16, v249\n"
        "v_and_b32 v79, 0xffff0000, v249\n"
        "v_mov_b32 v250, 0xffffff00\n"
        "s_add_u32 m0, %[ring], 9216\n"
        "s_add_u32 %[tmp], %[sow], 44032\n"
        "buffer_load_dwordx4 %[vw0], %[srdw], %[sow] offen lds\n"
        "buffer_load_dwordx4 %[vw1], %[srdw], %[sow] offen offset:1024 lds\n"
        "buffer_load_dwordx4 %[vw2], %[srdw], %[sow] offen offset:2048 lds\n"
        "s_cmp_eq_u32 %[tmp], %[sowe]\n"
        "s_cselect_b32 %[sow], %[sows], %[tmp]\n"
        "s_waitcnt vmcnt(6) lgkmcnt(7)\n"
        "ds_read_b64_tr_b16 a[84:85], %[tr0] offset:3072\n"
        "ds_read_b64_tr_b16 a[86:87], %[tr0] offset:3584\n"
        "ds_read_b64_tr_b16 a[88:89], %[tr1] offset:3072\n"
        "ds_read_b64_tr_b16 a[90:91], %[tr1] offset:3584\n"
        "ds_read_b64_tr_b16 a[92:93], %[tr2] offset:3072\n"
        "ds_read_b64_tr_b16 a[94:95], %[tr2] offset:3584\n"
        STAMP_ASM(3)
        "s_add_u32 m0, %[ring], 12288\n"
        "s_add_u32 %[tmp], %[sow], 44032\n"
        "buffer_load_dwordx4 %[vw0], %[srdw], %[sow] offen lds\n"
        "buffer_load_dwordx4 %[vw1], %[srdw], %[sow] offen offset:1024 lds\n"
        "buffer_load_dwordx4 %[vw2], %[srdw], %[sow] offen offset:2048 lds\n"
        "s_cmp_eq_u32 %[tmp], %[sowe]\n"
        "s_cselect_b32 %[sow], %[sows], %[tmp]\n"
        "s_waitcnt vmcnt(6) lgkmcnt(7)\n"
        "ds_read_b64_tr_b16 a[96:97], %[tr0] offset:6144\n"
        "ds_read_b64_tr_b16 a[98:99], %[tr0] offset:6656\n"
        "ds_read_b64_tr_b16 a[100:101], %[tr1] offset:6144\n"
        "ds_read_b64_tr_b16 a[102:103], %[tr1] offset:6656\n"
        "ds_read_b64_tr_b16 a[104:105], %[tr2] offset:6144\n"
        "ds_read_b64_tr_b16 a[106:107], %[tr2] offset:6656\n"
        "s_add_u32 m0, %[ring], 15360\n"
        "s_add_u32 %[tmp], %[sow], 44032\n"
        "buffer_load_dwordx4 %[vw0], %[srdw], %[sow] offen lds\n"
        "buffer_load_dwordx4 %[vw1], %[srdw], %[sow] offen offset:1024 lds\n"
        "buffer_load_dwordx4 %[vw2], %[srdw], %[sow] offen offset:2048 lds\n"
        "s_cmp_eq_u32 %[tmp], %[sowe]\n"
        "s_cselect_b32 %[sow], %[sows], %[tmp]\n"
        "s_waitcnt vmcnt(6) lgkmcnt(7)\n"
        "ds_read_b64_tr_b16 a[108:109], %[tr0] offset:9216\n"
        "ds_read_b64_tr_b16 a[110:111], %[tr0] offset:9728\n"
        "ds_read_b64_tr_b16 a[112:113], %[tr1] offset:9216\n"
        "ds_read_b64_tr_b16 a[114:115], %[tr1] offset:9728\n"
        "ds_read_b64_tr_b16 a[116:117], %[tr2] offset:9216\n"
        "ds_read_b64_tr_b16 a[118:119], %[tr2] offset:9728\n"
        "s_add_u32 m0, %[ringx], 0\n"
        "s_add_u32 %[tmp], %[sox], 131072\n"
        "buffer_load_dwordx4 %[vx0], %[srdx], %[sox] offen lds\n"
        "buffer_load_dwordx4 %[vx1], %[srdx], %[sox] offen offset:1024 lds\n"
        "buffer_load_dwordx4 %[vx2], %[srdx], %[sox] offen offset:2048 lds\n"
        "buffer_load_dwordx4 %[vx3], %[srdx], %[sox] offen offset:3072 lds\n"
        "s_cmp_eq_u32 %[tmp], %[soxe]\n"
        "s_cselect_b32 %[sox], %[soxs], %[tmp]\n"
        "s_waitcnt vmcnt(7) lgkmcnt(7)\n"
        "ds_read_b64_tr_b16 a[120:121], %[tr0] offset:12288\n"
        "ds_read_b64_tr_b16 a[122:123], %[tr0] offset:12800\n"
        "ds_read_b64_tr_b16 a[124:125], %[tr1] offset:12288\n"
        "ds_read_b64_tr_b16 a[126:127], %[tr1] offset:12800\n"
        "ds_read_b64_tr_b16 a[128:129], %[tr2] offset:12288\n"
        "ds_read_b64_tr_b16 a[130:131], %[tr2] offset:12800\n"
        "s_add_u32 m0, %[ringx], 4096\n"
        "s_add_u32 %[tmp], %[sox], 131072\n"
        "buffer_load_dwordx4 %[vx0], %[srdx], %[sox] offen lds\n"
        "buffer_load_dwordx4 %[vx1], %[srdx], %[sox] offen offset:1024 lds\n"
        "buffer_load_dwordx4 %[vx2], %[srdx], %[sox] offen offset:2048 lds\n"
        "buffer_load_dwordx4 %[vx3], %[srdx], %[sox] offen offset:3072 lds\n"
        "s_cmp_eq_u32 %[tmp], %[soxe]\n"
        "s_cselect_b32 %[sox], %[soxs], %[tmp]\n"
        "s_waitcnt vmcnt(8) lgkmcnt(7)\n"
        "ds_read_b64_tr_b16 a[132:133], %[tr0] offset:15360\n"
        "ds_read_b64_tr_b16 a[134:135], %[tr0] offset:15872\n"
        "ds_read_b64_tr_b16 a[136:137], %[tr1] offset:15360\n"
        "ds_read_b64_tr_b16 a[138:139], %[tr1] offset:15872\n"
        "ds_read_b64_tr_b16 a[140:141], %[tr2] offset:15360\n"
        "ds_read_b64_tr_b16 a[142:143], %[tr2] offset:15872\n"
        "s_add_u32 m0, %[ring], 0\n"
        "s_add_u32 %[tmp], %[sow], 44032\n"
        "buffer_load_dwordx4 %[vw0], %[srdw], %[sow] offen lds\n"
        "buffer_load_dwordx4 %[vw1], %[srdw], %[sow] offen offset:1024 lds\n"
        "buffer_load_dwordx4 %[vw2], %[srdw], %[sow] offen offset:2048 lds\n"
        "s_cmp_eq_u32 %[tmp], %[sowe]\n"
        "s_cselect_b32 %[sow], %[sows], %[tmp]\n"
        "s_waitcnt vmcnt(7) lgkmcnt(7)\n"
        "ds_read_b64_tr_b16 a[192:193], %[tqx0] offset:0\n"
        "ds_read_b64_tr_b16 a[194:195], %[tqx0] offset:1024\n"
        "ds_read_b64_tr_b16 a[196:197], %[tqx1] offset:0\n"
        "ds_read_b64_tr_b16 a[198:199], %[tqx1] offset:1024\n"
        "ds_read_b64_tr_b16 a[200:201], %[tqx0] offset:2048\n"
        "ds_read_b64_tr_b16 a[202:203], %[tqx0] offset:3072\n"
        "ds_read_b64_tr_b16 a[204:205], %[tqx1] offset:2048\n"
        "ds_read_b64_tr_b16 a[206:207], %[tqx1] offset:3072\n"
        "s_add_u32 m0, %[ring], 3072\n"
        "s_add_u32 %[tmp], %[sow], 44032\n"
        "buffer_load_dwordx4 %[vw0], %[srdw], %[sow] offen lds\n"
        "buffer_load_dwordx4 %[vw1], %[srdw], %[sow] offen offset:1024 lds\n"
        "buffer_load_dwordx4 %[vw2], %[srdw], %[sow] offen offset:2048 lds\n"
        "s_cmp_eq_u32 %[tmp], %[sowe]\n"
        "s_cselect_b32 %[sow], %[sows], %[tmp]\n"
        "buffer_load_dwordx4 v[236:239], %[vfl], %[srdc], 0 offen sc1\n"
        "s_waitcnt vmcnt(7) lgkmcnt(7)\n"
        "ds_read_b64_tr_b16 a[208:209], %[tqx0] offset:4096\n"
        "ds_read_b64_tr_b16 a[210:211], %[tqx0] offset:5120\n"
        "ds_read_b64_tr_b16 a[212:213], %[tqx1] offset:4096\n"
        "ds_read_b64_tr_b16 a[214:215], %[tqx1] offset:5120\n"
        "ds_read_b64_tr_b16 a[216:217], %[tqx0] offset:6144\n"
        "ds_read_b64_tr_b16 a[218:219], %[tqx0] offset:7168\n"
        "ds_read_b64_tr_b16 a[220:221], %[tqx1] offset:6144\n"
        "ds_read_b64_tr_b16 a[222:223], %[tqx1] offset:7168\n"
        "s_add_u32 m0, %[ring], 6144\n"
        "s_add_u32 %[tmp], %[sow], 44032\n"
        "buffer_load_dwordx4 %[vw0], %[srdw], %[sow] offen lds\n"
        "buffer_load_dwordx4 %[vw1], %[srdw], %[sow] offen offset:1024 lds\n"
        "buffer_load_dwordx4 %[vw2], %[srdw], %[sow] offen offset:2048 lds\n"
        "s_cmp_eq_u32 %[tmp], %[sowe]\n"
        "s_cselect_b32 %[sow], %[sows], %[tmp]\n"
        "s_waitcnt vmcnt(7) lgkmcnt(7)\n"
        "ds_read_b64_tr_b16 a[144:145], %[tr0] offset:0\n"
        "ds_read_b64_tr_b16 a[146:147], %[tr0] offset:512\n"
        "ds_read_b64_tr_b16 a[148:149], %[tr1] offset:0\n"
        "ds_read_b64_tr_b16 a[150:151], %[tr1] offset:512\n"
        "ds_read_b64_tr_b16 a[152:153], %[tr2] offset:0\n"
        "ds_read_b64_tr_b16 a[154:155], %[tr2] offset:512\n"
        "s_add_u32 m0, %[ring], 9216\n"
        "s_add_u32 %[tmp], %[sow], 44032\n"
        "buffer_load_dwordx4 %[vw0], %[srdw], %[sow] offen lds\n"
        "buffer_load_dwordx4 %[vw1], %[srdw], %[sow] offen offset:1024 lds\n"
        "buffer_load_dwordx4 %[vw2], %[srdw], %[sow] offen offset:2048 lds\n"
        "s_cmp_eq_u32 %[tmp], %[sowe]\n"
        "s_cselect_b32 %[sow], %[sows], %[tmp]\n"
        "s_waitcnt vmcnt(7) lgkmcnt(7)\n"
        "ds_read_b64_tr_b16 a[156:157], %[tr0] offset:3072\n"
        "ds_read_b64_tr_b16 a[158:159], %[tr0] offset:3584\n"
        "ds_read_b64_tr_b16 a[160:161], %[tr1] offset:3072\n"
        "ds_read_b64_tr_b16 a[162:163], %[tr1] offset:3584\n"
        "ds_read_b64_tr_b16 a[164:165], %[tr2] offset:3072\n"
        "ds_read_b64_tr_b16 a[166:167], %[tr2] offset:3584\n"
        "s_waitcnt vmcnt(6) lgkmcnt(0)\n"
        ".Lf1%=:\n"
        STAMP_ASM(4)
        "v_xor_b32 v240, %[tag], v236\n"
        "v_bitop3_b32 v240, v237, %[tag], v240 bitop3:0xbe\n"
        "v_bitop3_b32 v240, v238, %[tag], v240 bitop3:0xbe\n"
        "v_bitop3_b32 v240, v239, %[tag], v240 bitop3:0xbe\n"
        "v_cmp_ne_u32 vcc, 0, v240\n"
        "s_cbranch_vccz .Lp1%=\n"
        "s_mov_b32 %[tmp], 0x100000\n"
        ".Lp0%=:\n"
        "buffer_load_dwordx4 v[236:239], %[vfl], %[srdc], 0 offen sc1\n"
        "s_waitcnt vmcnt(0)\n"
        "v_xor_b32 v240, %[tag], v236\n"
        "v_bitop3_b32 v240, v237, %[tag], v240 bitop3:0xbe\n"
        "v_bitop3_b32 v240, v238, %[tag], v240 bitop3:0xbe\n"
        "v_bitop3_b32 v240, v239, %[tag], v240 bitop3:0xbe\n"
        "v_cmp_ne_u32 vcc, 0, v240\n"
        "s_cbranch_vccz .Lp1%=\n"
        "s_sub_u32 %[tmp], %[tmp], 1\n"
        "s_cbranch_scc0 .Lp0%=\n"
        ".Lp1%=:\n"
        STAMP_ASM(5)
        "buffer_load_dwordx4 v[160:163], %[vhs], %[srda], %[soa] offen sc1\n"
        "s_add_u32 %[soa], %[soa], 2048\n"
        "s_cmp_eq_u32 %[soa], %[soae]\n"
        "s_cselect_b32 %[soa], %[soas], %[soa]\n"
        "buffer_load_dwordx4 v[164:167], %[vhs], %[srda], %[soa] offen sc1\n"
        "s_add_u32 %[soa], %[soa], 2048\n"
        "s_cmp_eq_u32 %[soa], %[soae]\n"
        "s_cselect_b32 %[soa], %[soas], %[soa]\n"
        "buffer_load_dwordx4 v[168:171], %[vhs], %[srda], %[soa] offen sc1\n"
        "s_add_u32 %[soa], %[soa], 2048\n"
        "s_cmp_eq_u32 %[soa], %[soae]\n"
        "s_cselect_b32 %[soa], %[soas], %[soa]\n"
        "buffer_load_dwordx4 v[172:175], %[vhs], %[srda], %[soa] offen sc1\n"
        "s_add_u32 %[soa], %[soa], 2048\n"
        "s_cmp_eq_u32 %[soa], %[soae]\n"
        "s_cselect_b32 %[soa], %[soas], %[soa]\n"
        "buffer_load_dwordx4 v[176:179], %[vhs], %[srda], %[soa] offen sc1\n"
        "s_add_u32 %[soa], %[soa], 2048\n"
        "s_cmp_eq_u32 %[soa], %[soae]\n"
        "s_cselect_b32 %[soa], %[soas], %[soa]\n"
        "buffer_load_dwordx4 v[180:183], %[vhs], %[srda], %[soa] offen sc1\n"
        "s_add_u32 %[soa], %[soa], 2048\n"
        "s_cmp_eq_u32 %[soa], %[soae]\n"
        "s_cselect_b32 %[soa], %[soas], %[soa]\n"
        "buffer_load_dwordx4 v[184:187], %[vhs], %[srda], %[soa] offen sc1\n"
        "s_add_u32 %[soa], %[soa], 2048\n"
        "s_cmp_eq_u32 %[soa], %[soae]\n"
        "s_cselect_b32 %[soa], %[soas], %[soa]\n"
        "buffer_load_dwordx4 v[188:191], %[vhs], %[srda], %[soa] offen sc1\n"
        "s_add_u32 %[soa], %[soa], 2048\n"
        "s_cmp_eq_u32 %[soa], %[soae]\n"
        "s_cselect_b32 %[soa], %[soas], %[soa]\n"
        "buffer_load_dwordx4 v[192:195], %[vhs], %[srda], %[soa] offen sc1\n"
        "s_add_u32 %[soa], %[soa], 2048\n"
        "s_cmp_eq_u32 %[soa], %[soae]\n"
        "s_cselect_b32 %[soa], %[soas], %[soa]\n"
        "buffer_load_dwordx4 v[196:199], %[vhs], %[srda], %[soa] offen sc1\n"
        "s_add_u32 %[soa], %[soa], 2048\n"
        "s_cmp_eq_u32 %[soa], %[soae]\n"
        "s_cselect_b32 %[soa], %[soas], %[soa]\n"
        "buffer_load_dwordx4 v[200:203], %[vhs], %[srda], %[soa] offen sc1\n"
        "s_add_u32 %[soa], %[soa], 2048\n"
        "s_cmp_eq_u32 %[soa], %[soae]\n"
        "s_cselect_b32 %[soa], %[soas], %[soa]\n"
        "buffer_load_dwordx4 v[204:207], %[vhs], %[srda], %[soa] offen sc1\n"
        "s_add_u32 %[soa], %[soa], 2048\n"
        "s_cmp_eq_u32 %[soa], %[soae]\n"
        "s_cselect_b32 %[soa], %[soas], %[soa]\n"
        "buffer_load_dwordx4 v[208:211], %[vhs], %[srda], %[soa] offen sc1\n"
        "s_add_u32 %[soa], %[soa], 2048\n"
        "s_cmp_eq_u32 %[soa], %[soae]\n"
        "s_cselect_b32 %[soa], %[soas], %[soa]\n"
        "buffer_load_dwordx4 v[212:215], %[vhs], %[srda], %[soa] offen sc1\n"
        "s_add_u32 %[soa], %[soa], 2048\n"
        "s_cmp_eq_u32 %[soa], %[soae]\n"
        "s_cselect_b32 %[soa], %[soas], %[soa]\n"
        "buffer_load_dwordx4 v[216:219], %[vhs], %[srda], %[soa] offen sc1\n"
        "s_add_u32 %[soa], %[soa], 2048\n"
        "s_cmp_eq_u32 %[soa], %[soae]\n"
        "s_cselect_b32 %[soa], %[soas], %[soa]\n"
        "buffer_load_dwordx4 v[220:223], %[vhs], %[srda], %[soa] offen sc1\n"
        "s_add_u32 %[soa], %[soa], 2048\n"
        "s_cmp_eq_u32 %[soa], %[soae]\n"
        "s_cselect_b32 %[soa], %[soas], %[soa]\n"
        "s_waitcnt vmcnt(19)\n"
        "ds_read_b64_tr_b16 a[168:169], %[tr0] offset:6144\n"
        "ds_read_b64_tr_b16 a[170:171], %[tr0] offset:6656\n"
        "ds_read_b64_tr_b16 a[172:173], %[tr1] offset:6144\n"
        "ds_read_b64_tr_b16 a[174:175], %[tr1] offset:6656\n"
        "ds_read_b64_tr_b16 a[176:177], %[tr2] offset:6144\n"
        "ds_read_b64_tr_b16 a[178:179], %[tr2] offset:6656\n"
        "s_waitcnt vmcnt(16)\n"
        "ds_read_b64_tr_b16 a[180:181], %[tr0] offset:9216\n"
        "ds_read_b64_tr_b16 a[182:183], %[tr0] offset:9728\n"
        "ds_read_b64_tr_b16 a[184:185], %[tr1] offset:9216\n"
        "ds_read_b64_tr_b16 a[186:187], %[tr1] offset:9728\n"
        "ds_read_b64_tr_b16 a[188:189], %[tr2] offset:9216\n"
        "ds_read_b64_tr_b16 a[190:191], %[tr2] offset:9728\n"
        "s_waitcnt vmcnt(15)\n"
        "v_mfma_f32_32x32x16_bf16 v[112:127], a[0:3], v[160:163], 0\n"
        "v_mfma_f32_32x32x16_bf16 v[128:143], a[4:7], v[160:163], 0\n"
        "v_mfma_f32_32x32x16_bf16 v[144:159], a[8:11], v[160:163], 0\n"
        "buffer_load_dwordx4 v[160:163], %[vhk], %[srda], %[soa] offen sc1\n"
        "s_add_u32 %[soa], %[soa], 2048\n"
        "s_cmp_eq_u32 %[soa], %[soae]\n"
        "s_cselect_b32 %[soa], %[soas], %[soa]\n"
        "s_waitcnt vmcnt(15)\n"
        "v_mfma_f32_32x32x16_bf16 v[112:127], a[12:15], v[164:167], v[112:127]\n"
        "v_mfma_f32_32x32x16_bf16 v[128:143], a[16:19], v[164:167], v[128:143]\n"
        "v_mfma_f32_32x32x16_bf16 v[144:159], a[20:23], v[164:167], v[144:159]\n"
        "buffer_load_dwordx4 v[164:167], %[vhk], %[srda], %[soa] offen sc1\n"
        "s_add_u32 %[soa], %[soa], 2048\n"
        "s_cmp_eq_u32 %[soa], %[soae]\n"
        "s_cselect_b32 %[soa], %[soas], %[soa]\n"
        "s_waitcnt vmcnt(15)\n"
        "v_mfma_f32_32x32x16_bf16 v[112:127], a[24:27], v[168:171], v[112:127]\n"
        "v_mfma_f32_32x32x16_bf16 v[128:143], a[28:31], v[168:171], v[128:143]\n"
        "v_mfma_f32_32x32x16_bf16 v[144:159], a[32:35], v[168:171], v[144:159]\n"
        "buffer_load_dwordx4 v[168:171], %[vhk], %[srda], %[soa] offen sc1\n"
        "s_add_u32 %[soa], %[soa], 2048\n"
        "s_cmp_eq_u32 %[soa], %[soae]\n"
        "s_cselect_b32 %[soa], %[soas], %[soa]\n"
        "s_waitcnt vmcnt(15)\n"
        "v_mfma_f32_32x32x16_bf16 v[112:127], a[36:39], v[172:175], v[112:127]\n"
        "v_mfma_f32_32x32x16_bf16 v[128:143], a[40:43], v[172:175], v[128:143]\n"
        "v_mfma_f32_32x32x16_bf16 v[144:159], a[44:47], v[172:175], v[144:159]\n"
        "buffer_load_dwordx4 v[172:175], %[vhk], %[srda], %[soa] offen sc1\n"
        "s_add_u32 %[soa], %[soa], 2048\n"
        "s_cmp_eq_u32 %[soa], %[soae]\n"
        "s_cselect_b32 %[soa], %[soas], %[soa]\n"
        "s_waitcnt vmcnt(15)\n"
        "v_mfma_f32_32x32x16_bf16 v[112:127], a[48:51], v[176:179], v[112:127]\n"
        "v_mfma_f32_32x32x16_bf16 v[128:143], a[52:55], v[176:179], v[128:143]\n"
        "v_mfma_f32_32x32x16_bf16 v[144:159], a[56:59], v[176:179], v[144:159]\n"
        "buffer_load_dwordx4 v[176:179], %[vhk], %[srda], %[soa] offen sc1\n"
        "s_add_u32 %[soa], %[soa], 2048\n"
        "s_cmp_eq_u32 %[soa], %[soae]\n"
        "s_cselect_b32 %[soa], %[soas], %[soa]\n"
        "s_waitcnt vmcnt(15)\n"
        "v_mfma_f32_32x32x16_bf16 v[112:127], a[60:63], v[180:183], v[112:127]\n"
        "v_mfma_f32_32x32x16_bf16 v[128:143], a[64:67], v[180:183], v[128:143]\n"
        "v_mfma_f32_32x32x16_bf16 v[144:159], a[68:71], v[180:183], v[144:159]\n"
        "buffer_load_dwordx4 v[180:183], %[vhk], %[srda], %[soa] offen sc1\n"
        "s_add_u32 %[soa], %[soa], 2048\n"
        "s_cmp_eq_u32 %[soa], %[soae]\n"
        "s_cselect_b32 %[soa], %[soas], %[soa]\n"
        "s_waitcnt vmcnt(15)\n"
        "v_mfma_f32_32x32x16_bf16 v[112:127], a[72:75], v[184:187], v[112:127]\n"
        "v_mfma_f32_32x32x16_bf16 v[128:143], a[76:79], v[184:187], v[128:143]\n"
        "v_mfma_f32_32x32x16_bf16 v[144:159], a[80:83], v[184:187], v[144:159]\n"
        "buffer_load_dwordx4 v[184:187], %[vhk], %[srda], %[soa] offen sc1\n"
        "s_add_u32 %[soa], %[soa], 2048\n"
        "s_cmp_eq_u32 %[soa], %[soae]\n"
        "s_cselect_b32 %[soa], %[soas], %[soa]\n"
        "s_waitcnt vmcnt(15)\n"
        "v_mfma_f32_32x32x16_bf16 v[112:127], a[84:87], v[188:191], v[112:127]\n"
        "v_mfma_f32_32x32x16_bf16 v[128:143], a[88:91], v[188:191], v[128:143]\n"
        "v_mfma_f32_32x32x16_bf16 v[144:159], a[92:95], v[188:191], v[144:159]\n"
        "buffer_load_dwordx4 v[188:191], %[vhk], %[srda], %[soa] offen sc1\n"
        "s_add_u32 %[soa], %[soa], 2048\n"
        "s_cmp_eq_u32 %[soa], %[soae]\n"
        "s_cselect_b32 %[soa], %[soas], %[soa]\n"
        "s_waitcnt vmcnt(15)\n"
        "v_mfma_f32_32x32x16_bf16 v[112:127], a[96:99], v[192:195], v[112:127]\n"
        "v_mfma_f32_32x32x16_bf16 v[128:143], a[100:103], v[192:195], v[128:143]\n"
        "v_mfma_f32_32x32x16_bf16 v[144:159], a[104:107], v[192:195], v[144:159]\n"
        "buffer_load_dwordx4 v[192:195], %[vhk], %[srda], %[soa] offen sc1\n"
        "s_add_u32 %[soa], %[soa], 2048\n"
        "s_cmp_eq_u32 %[soa], %[soae]\n"
        "s_cselect_b32 %[soa], %[soas], %[soa]\n"
        "s_waitcnt vmcnt(15)\n"
        "v_mfma_f32_32x32x16_bf16 v[112:127], a[108:111], v[196:199], v[112:127]\n"
        "v_mfma_f32_32x32x16_bf16 v[128:143], a[112:115], v[196:199], v[128:143]\n"
        "v_mfma_f32_32x32x16_bf16 v[144:159], a[116:119], v[196:199], v[144:159]\n"
        "buffer_load_dwordx4 v[196:199], %[vhk], %[srda], %[soa] offen sc1\n"
        "s_add_u32 %[soa], %[soa], 2048\n"
        "s_cmp_eq_u32 %[soa], %[soae]\n"
        "s_cselect_b32 %[soa], %[soas], %[soa]\n"
        "s_waitcnt vmcnt(15)\n"
        "v_mfma_f32_32x32x16_bf16 v[112:127], a[120:123], v[200:203], v[112:127]\n"
        "v_mfma_f32_32x32x16_bf16 v[128:143], a[124:127], v[200:203], v[128:143]\n"
        "v_mfma_f32_32x32x16_bf16 v[144:159], a[128:131], v[200:203], v[144:159]\n"
        "buffer_load_dwordx4 v[200:203], %[vhk], %[srda], %[soa] offen sc1\n"
        "s_add_u32 %[soa], %[soa], 2048\n"
        "s_cmp_eq_u32 %[soa], %[soae]\n"
        "s_cselect_b32 %[soa], %[soas], %[soa]\n"
        "s_waitcnt vmcnt(15)\n"
        "v_mfma_f32_32x32x16_bf16 v[112:127], a[132:135], v[204:207], v[112:127]\n"
        "v_mfma_f32_32x32x16_bf16 v[128:143], a[136:139], v[204:207], v[128:143]\n"
        "v_mfma_f32_32x32x16_bf16 v[144:159], a[140:143], v[204:207], v[144:159]\n"
        "buffer_load_dwordx4 v[204:207], %[vhk], %[srda], %[soa] offen sc1\n"
        "s_add_u32 %[soa], %[soa], 2048\n"
        "s_cmp_eq_u32 %[soa], %[soae]\n"
        "s_cselect_b32 %[soa], %[soas], %[soa]\n"
        "s_waitcnt vmcnt(15)\n"
        "v_mfma_f32_32x32x16_bf16 v[112:127], a[144:147], v[208:211], v[112:127]\n"
        "v_mfma_f32_32x32x16_bf16 v[128:143], a[148:151], v[208:211], v[128:143]\n"
        "v_mfma_f32_32x32x16_bf16 v[144:159], a[152:155], v[208:211], v[144:159]\n"
        "buffer_load_dwordx4 v[208:211], %[vhk], %[srda], %[soa] offen sc1\n"
        "s_add_u32 %[soa], %[soa], 2048\n"
        "s_cmp_eq_u32 %[soa], %[soae]\n"
        "s_cselect_b32 %[soa], %[soas], %[soa]\n"
        "s_waitcnt vmcnt(15)\n"
        "v_mfma_f32_32x32x16_bf16 v[112:127], a[156:159], v[212:215], v[112:127]\n"
        "v_mfma_f32_32x32x16_bf16 v[128:143], a[160:163], v[212:215], v[128:143]\n"
        "v_mfma_f32_32x32x16_bf16 v[144:159], a[164:167], v[212:215], v[144:159]\n"
        "buffer_load_dwordx4 v[212:215], %[vhk], %[srda], %[soa] offen sc1\n"
        "s_add_u32 %[soa], %[soa], 2048\n"
        "s_cmp_eq_u32 %[soa], %[soae]\n"
        "s_cselect_b32 %[soa], %[soas], %[soa]\n"
        "s_waitcnt vmcnt(15) lgkmcnt(6)\n"
        "v_mfma_f32_32x32x16_bf16 v[112:127], a[168:171], v[216:219], v[112:127]\n"
        "v_mfma_f32_32x32x16_bf16 v[128:143], a[172:175], v[216:219], v[128:143]\n"
        "v_mfma_f32_32x32x16_bf16 v[144:159], a[176:179], v[216:219], v[144:159]\n"
        "buffer_load_dwordx4 v[216:219], %[vhk], %[srda], %[soa] offen sc1\n"
        "s_add_u32 %[soa], %[soa], 2048\n"
        "s_cmp_eq_u32 %[soa], %[soae]\n"
        "s_cselect_b32 %[soa], %[soas], %[soa]\n"
        "s_waitcnt vmcnt(15) lgkmcnt(0)\n"
        "v_mfma_f32_32x32x16_bf16 v[112:127], a[180:183], v[220:223], v[112:127]\n"
        "v_mfma_f32_32x32x16_bf16 v[128:143], a[184:187], v[220:223], v[128:143]\n"
        "v_mfma_f32_32x32x16_bf16 v[144:159], a[188:191], v[220:223], v[144:159]\n"
        "buffer_load_dwordx4 v[220:223], %[vhk], %[srda], %[soa] offen sc1\n"
        "s_add_u32 %[soa], %[soa], 2048\n"
        "s_cmp_eq_u32 %[soa], %[soae]\n"
        "s_cselect_b32 %[soa], %[soas], %[soa]\n"
        STAMP_ASM(6)
        "buffer_load_dwordx4 v[252:255], %[vf2], %[srdc], 0 offen sc1\n"
        "s_waitcnt vmcnt(16)\n"
        "v_mfma_f32_32x32x16_bf16 v[64:79], a[0:3], v[160:163], v[64:79]\n"
        "s_nop 10\n"
        "s_cmp_lg_u32 %[wv], 3\n"
        "s_cbranch_scc1 .Lo1%=\n"
        "ds_write_b128 %[xa0], v[112:115] offset:0\n"
        "ds_write_b128 %[xa0], v[116:119] offset:1024\n"
        "ds_write_b128 %[xa0], v[120:123] offset:2048\n"
        "ds_write_b128 %[xa0], v[124:127] offset:3072\n"
        ".Lo1%=:\n"
        "ds_write_b128 %[xa1], v[128:131] offset:0\n"
        "v_mfma_f32_32x32x16_bf16 v[80:95], a[4:7], v[160:163], 0\n"
        "ds_write_b128 %[xa1], v[132:135] offset:1024\n"
        "ds_write_b128 %[xa1], v[136:139] offset:2048\n"
        "v_mfma_f32_32x32x16_bf16 v[96:111], a[8:11], v[160:163], 0\n"
        "ds_write_b128 %[xa1], v[140:143] offset:3072\n"
        "ds_write_b128 %[xa2], v[144:147] offset:0\n"
        "ds_write_b128 %[xa2], v[148:151] offset:1024\n"
        "s_waitcnt vmcnt(15)\n"
        "v_mfma_f32_32x32x16_bf16 v[64:79], a[12:15], v[164:167], v[64:79]\n"
        "ds_write_b128 %[xa2], v[152:155] offset:2048\n"
        "ds_write_b128 %[xa2], v[156:159] offset:3072\n"
        "v_mfma_f32_32x32x16_bf16 v[80:95], a[16:19], v[164:167], v[80:95]\n"
        "v_mfma_f32_32x32x16_bf16 v[96:111], a[20:23], v[164:167], v[96:111]\n"
        "s_waitcnt lgkmcnt(0)\n"
        "s_barrier\n"
        "ds_read_b128 v[128:131], %[xr] offset:0\n"
        "ds_read_b128 v[132:135], %[xr] offset:1024\n"
        "ds_read_b128 v[136:139], %[xr] offset:2048\n"
        "ds_read_b128 v[140:143], %[xr] offset:3072\n"
        "ds_read_b128 v[144:147], %[xr] offset:4096\n"
        "ds_read_b128 v[148:151], %[xr] offset:5120\n"
        "ds_read_b128 v[152:155], %[xr] offset:6144\n"
        "ds_read_b128 v[156:159], %[xr] offset:7168\n"
        "ds_read_b128 v[224:227], %[xr] offset:8192\n"
        "ds_read_b128 v[228:231], %[xr] offset:9216\n"
        "ds_read_b128 v[232:235], %[xr] offset:10240\n"
        "ds_read_b128 v[236:239], %[xr] offset:11264\n"
        "s_waitcnt vmcnt(14)\n"
        "v_mfma_f32_32x32x16_bf16 v[64:79], a[24:27], v[168:171], v[64:79]\n"
        "s_add_u32 m0, %[ring], 12288\n"
        "s_add_u32 %[tmp], %[sox], 131072\n"
        "buffer_load_dwordx4 %[vx0], %[srdx], %[sox] offen lds\n"
        "s_waitcnt lgkmcnt(8)\n"
        "v_add_f32 v112, v112, v128\n"
        "v_mfma_f32_32x32x16_bf16 v[80:95], a[28:31], v[168:171], v[80:95]\n"
        "v_add_f32 v113, v113, v129\n"
        "v_add_f32 v114, v114, v130\n"
        "v_add_f32 v115, v115, v131\n"
        "v_add_f32 v116, v116, v132\n"
        "v_add_f32 v117, v117, v133\n"
        "v_add_f32 v118, v118, v134\n"
        "v_add_f32 v119, v119, v135\n"
        "v_add_f32 v120, v120, v136\n"
        "v_mfma_f32_32x32x16_bf16 v[96:111], a[32:35], v[168:171], v[96:111]\n"
        "buffer_load_dwordx4 %[vx1], %[srdx], %[sox] offen offset:1024 lds\n"
        "v_add_f32 v121, v121, v137\n"
        "v_add_f32 v122, v122, v138\n"
        "v_add_f32 v123, v123, v139\n"
        "v_add_f32 v124, v124, v140\n"
        "v_add_f32 v125, v125, v141\n"
        "v_add_f32 v126, v126, v142\n"
        "v_add_f32 v127, v127, v143\n"
        "s_waitcnt lgkmcnt(4)\n"
        "s_waitcnt vmcnt(15)\n"
        "v_mfma_f32_32x32x16_bf16 v[64:79], a[36:39], v[172:175], v[64:79]\n"
        "v_add_f32 v112, v112, v144\n"
        "v_add_f32 v113, v113, v145\n"
        "v_add_f32 v114, v114, v146\n"
        "v_add_f32 v115, v115, v147\n"
        "v_add_f32 v116, v116, v148\n"
        "v_add_f32 v117, v117, v149\n"
        "v_add_f32 v118, v118, v150\n"
        "v_add_f32 v119, v119, v151\n"
        "v_mfma_f32_32x32x16_bf16 v[80:95], a[40:43], v[172:175], v[80:95]\n"
        "buffer_load_dwordx4 %[vx2], %[srdx], %[sox] offen offset:2048 lds\n"
        "v_add_f32 v120, v120, v152\n"
        "v_add_f32 v121, v121, v153\n"
        "v_add_f32 v122, v122, v154\n"
        "v_add_f32 v123, v123, v155\n"
        "v_add_f32 v124, v124, v156\n"
        "v_add_f32 v125, v125, v157\n"
        "v_add_f32 v126, v126, v158\n"
        "v_add_f32 v127, v127, v159\n"
        "v_mfma_f32_32x32x16_bf16 v[96:111], a[44:47], v[172:175], v[96:111]\n"
        "s_waitcnt lgkmcnt(0)\n"
        "v_add_f32 v112, v112, v224\n"
        "v_add_f32 v113, v113, v225\n"
        "v_add_f32 v114, v114, v226\n"
        "v_add_f32 v115, v115, v227\n"
        "v_add_f32 v116, v116, v228\n"
        "v_add_f32 v117, v117, v229\n"
        "v_add_f32 v118, v118, v230\n"
        "s_waitcnt vmcnt(15)\n"
        "v_mfma_f32_32x32x16_bf16 v[64:79], a[48:51], v[176:179], v[64:79]\n"
        "buffer_load_dwordx4 %[vx3], %[srdx], %[sox] offen offset:3072 lds\n"
        "s_cmp_eq_u32 %[tmp], %[soxe]\n"
        "s_cselect_b32 %[sox], %[soxs], %[tmp]\n"
        "v_add_f32 v119, v119, v231\n"
        "v_add_f32 v120, v120, v232\n"
        "v_add_f32 v121, v121, v233\n"
        "v_add_f32 v122, v122, v234\n"
        "v_add_f32 v123, v123, v235\n"
        "v_add_f32 v124, v124, v236\n"
        "v_add_f32 v125, v125, v237\n"
        "v_add_f32 v126, v126, v238\n"
        "v_mfma_f32_32x32x16_bf16 v[80:95], a[52:55], v[176:179], v[80:95]\n"
        "v_add_f32 v127, v127, v239\n"
        "v_and_or_b32 v112, v112, v250, %[tb0]\n"
        "v_and_or_b32 v113, v113, v250, %[tb1]\n"
        "v_and_or_b32 v114, v114, v250, %[tb2]\n"
        "v_and_or_b32 v115, v115, v250, %[tb3]\n"
        "buffer_store_dwordx4 v[112:115], %[vl16], %[srds], 0 offen offset:0 sc1\n"
        "v_and_or_b32 v116, v116, v250, %[tb0]\n"
        "v_and_or_b32 v117, v117, v250, %[tb1]\n"
        "v_mfma_f32_32x32x16_bf16 v[96:111], a[56:59], v[176:179], v[96:111]\n"
        "v_and_or_b32 v118, v118, v250, %[tb2]\n"
        "v_and_or_b32 v119, v119, v250, %[tb3]\n"
        "buffer_store_dwordx4 v[116:119], %[vl16], %[srds], 0 offen offset:1024 sc1\n"
        "v_and_or_b32 v120, v120, v250, %[tb0]\n"
        "v_and_or_b32 v121, v121, v250, %[tb1]\n"
        "v_and_or_b32 v122, v122, v250, %[tb2]\n"
        "v_and_or_b32 v123, v123, v250, %[tb3]\n"
        "buffer_store_dwordx4 v[120:123], %[vl16], %[srds], 0 offen offset:2048 sc1\n"
        "s_waitcnt vmcnt(18)\n"
        "v_mfma_f32_32x32x16_bf16 v[64:79], a[60:63], v[180:183], v[64:79]\n"
        "v_and_or_b32 v124, v124, v250, %[tb0]\n"
        "v_and_or_b32 v125, v125, v250, %[tb1]\n"
        "v_and_or_b32 v126, v126, v250, %[tb2]\n"
        "v_and_or_b32 v127, v127, v250, %[tb3]\n"
        "buffer_store_dwordx4 v[124:127], %[vl16], %[srds], 0 offen offset:3072 sc1\n"
        "v_mfma_f32_32x32x16_bf16 v[80:95], a[64:67], v[180:183], v[80:95]\n"
        "v_mfma_f32_32x32x16_bf16 v[96:111], a[68:71], v[180:183], v[96:111]\n"
        "s_waitcnt vmcnt(18)\n"
        "v_mfma_f32_32x32x16_bf16 v[64:79], a[72:75], v[184:187], v[64:79]\n"
        "v_mfma_f32_32x32x16_bf16 v[80:95], a[76:79], v[184:187], v[80:95]\n"
        "v_mfma_f32_32x32x16_bf16 v[96:111], a[80:83], v[184:187], v[96:111]\n"
        "s_waitcnt vmcnt(17)\n"
        "v_mfma_f32_32x32x16_bf16 v[64:79], a[84:87], v[188:191], v[64:79]\n"
        "v_mfma_f32_32x32x16_bf16 v[80:95], a[88:91], v[188:191], v[80:95]\n"
        "v_mfma_f32_32x32x16_bf16 v[96:111], a[92:95], v[188:191], v[96:111]\n"
        "s_waitcnt vmcnt(16)\n"
        "v_mfma_f32_32x32x16_bf16 v[64:79], a[96:99], v[192:195], v[64:79]\n"
        "v_mfma_f32_32x32x16_bf16 v[80:95], a[100:103], v[192:195], v[80:95]\n"
        "v_mfma_f32_32x32x16_bf16 v[96:111], a[104:107], v[192:195], v[96:111]\n"
        "s_waitcnt vmcnt(15)\n"
        "v_mfma_f32_32x32x16_bf16 v[64:79], a[108:111], v[196:199], v[64:79]\n"
        "v_mfma_f32_32x32x16_bf16 v[80:95], a[112:115], v[196:199], v[80:95]\n"
        "v_mfma_f32_32x32x16_bf16 v[96:111], a[116:119], v[196:199], v[96:111]\n"
        "s_waitcnt vmcnt(14)\n"
        "v_mfma_f32_32x32x16_bf16 v[64:79], a[120:123], v[200:203], v[64:79]\n"
        "s_waitcnt vmcnt(4)\n"
        "ds_read_b64_tr_b16 a[224:225], %[tq0] offset:12288\n"
        "ds_read_b64_tr_b16 a[226:227], %[tq0] offset:13312\n"
        "ds_read_b64_tr_b16 a[228:229], %[tq1] offset:12288\n"
        "v_mfma_f32_32x32x16_bf16 v[80:95], a[124:127], v[200:203], v[80:95]\n"
        "ds_read_b64_tr_b16 a[230:231], %[tq1] offset:13312\n"
        "ds_read_b64_tr_b16 a[232:233], %[tq0] offset:14336\n"
        "ds_read_b64_tr_b16 a[234:235], %[tq0] offset:15360\n"
        "v_mfma_f32_32x32x16_bf16 v[96:111], a[128:131], v[200:203], v[96:111]\n"
        "ds_read_b64_tr_b16 a[236:237], %[tq1] offset:14336\n"
        "ds_read_b64_tr_b16 a[238:239], %[tq1] offset:15360\n"
        "s_waitcnt vmcnt(13)\n"
        "v_mfma_f32_32x32x16_bf16 v[64:79], a[132:135], v[204:207], v[64:79]\n"
        "v_mfma_f32_32x32x16_bf16 v[80:95], a[136:139], v[204:207], v[80:95]\n"
        "v_mfma_f32_32x32x16_bf16 v[96:111], a[140:143], v[204:207], v[96:111]\n"
        "s_waitcnt vmcnt(12)\n"
        "v_mfma_f32_32x32x16_bf16 v[64:79], a[144:147], v[208:211], v[64:79]\n"
        "v_mfma_f32_32x32x16_bf16 v[80:95], a[148:151], v[208:211], v[80:95]\n"
        "v_mfma_f32_32x32x16_bf16 v[96:111], a[152:155], v[208:211], v[96:111]\n"
        "s_waitcnt vmcnt(11)\n"
        "v_mfma_f32_32x32x16_bf16 v[80:95], a[160:163], v[212:215], v[80:95]\n"
        "v_mfma_f32_32x32x16_bf16 v[96:111], a[164:167], v[212:215], v[96:111]\n"
        "s_waitcnt vmcnt(10)\n"
        "v_mfma_f32_32x32x16_bf16 v[80:95], a[172:175], v[216:219], v[80:95]\n"
        "v_mfma_f32_32x32x16_bf16 v[96:111], a[176:179], v[216:219], v[96:111]\n"
        "s_waitcnt vmcnt(9)\n"
        "v_mfma_f32_32x32x16_bf16 v[80:95], a[184:187], v[220:223], v[80:95]\n"
        "v_mfma_f32_32x32x16_bf16 v[96:111], a[188:191], v[220:223], v[96:111]\n"
        "buffer_load_dwordx4 v[128:131], %[vl16], %[srdr], 0 offen offset:0 sc1\n"
        "buffer_load_dwordx4 v[132:135], %[vl16], %[srdr], 0 offen offset:1024 sc1\n"
        "buffer_load_dwordx4 v[136:139], %[vl16], %[srdr], 0 offen offset:2048 sc1\n"
        "buffer_load_dwordx4 v[140:143], %[vl16], %[srdr], 0 offen offset:3072 sc1\n"
        "v_mfma_f32_32x32x16_bf16 v[64:79], a[156:159], v[212:215], v[64:79]\n"
        "s_nop 9\n"
        "ds_write_b128 %[xa1], v[80:83] offset:36864\n"
        "ds_write_b128 %[xa1], v[84:87] offset:37888\n"
        "ds_write_b128 %[xa1], v[88:91] offset:38912\n"
        "v_mfma_f32_32x32x16_bf16 v[64:79], a[168:171], v[216:219], v[64:79]\n"
        "ds_write_b128 %[xa1], v[92:95] offset:39936\n"
        "ds_write_b128 %[xa2], v[96:99] offset:36864\n"
        "ds_write_b128 %[xa2], v[100:103] offset:37888\n"
        "v_mfma_f32_32x32x16_bf16 v[64:79], a[180:183], v[220:223], v[64:79]\n"
        "ds_write_b128 %[xa2], v[104:107] offset:38912\n"
        "s_waitcnt lgkmcnt(8)\n"
        "ds_write_b128 %[xa2], v[108:111] offset:39936\n"
        "s_cmp_lg_u32 %[wv], 3\n"
        "s_cbranch_scc1 .Lo0%=\n"
        "s_waitcnt vmcnt(12)\n"
        "s_mov_b32 %[tmp], 0x100000\n"
        ".Lq0%=:\n"
        "v_xor_b32 v241, %[tag], v252\n"
        "v_bitop3_b32 v241, v253, %[tag], v241 bitop3:0xbe\n"
        "v_bitop3_b32 v241, v254, %[tag], v241 bitop3:0xbe\n"
        "v_bitop3_b32 v241, v255, %[tag], v241 bitop3:0xbe\n"
        "v_cmp_ne_u32 vcc, 0, v241\n"
        "s_cbranch_vccz .Lq1%=\n"
        "buffer_load_dwordx4 v[252:255], %[vf2], %[srdc], 0 offen sc1\n"
        "s_sub_u32 %[tmp], %[tmp], 1\n"
        "s_waitcnt vmcnt(0)\n"
        "s_cbranch_scc0 .Lq0%=\n"
        ".Lq1%=:\n"
        "s_nop 3\n"
        "s_waitcnt lgkmcnt(8)\n"
        "ds_write_b128 %[xa0], v[64:67] offset:36864\n"
        "ds_write_b128 %[xa0], v[68:71] offset:37888\n"
        "ds_write_b128 %[xa0], v[72:75] offset:38912\n"
        "ds_write_b128 %[xa0], v[76:79] offset:39936\n"
        "s_waitcnt lgkmcnt(0)\n"
        "s_barrier\n"
        "s_add_u32 m0, %[ring], 4294922240\n"
        "s_sub_u32 %[tmp], %[sox], 4325376\n"
        "buffer_load_dwordx4 %[vx0], %[srdx], %[tmp] offen lds\n"
        "buffer_load_dwordx4 %[vx1], %[srdx], %[tmp] offen offset:1024 lds\n"
        "buffer_load_dwordx4 %[vx2], %[srdx], %[tmp] offen offset:2048 lds\n"
        "buffer_load_dwordx4 %[vx3], %[srdx], %[tmp] offen offset:3072 lds\n"
        "s_add_u32 m0, %[ring], 4294942720\n"
        "s_sub_u32 %[tmp], %[sox], 2883584\n"
        "buffer_load_dwordx4 %[vx0], %[srdx], %[tmp] offen lds\n"
        "buffer_load_dwordx4 %[vx1], %[srdx], %[tmp] offen offset:1024 lds\n"
        "buffer_load_dwordx4 %[vx2], %[srdx], %[tmp] offen offset:2048 lds\n"
        "buffer_load_dwordx4 %[vx3], %[srdx], %[tmp] offen offset:3072 lds\n"
        "s_add_u32 m0, %[ring], 4294963200\n"
        "s_sub_u32 %[tmp], %[sox], 1441792\n"
        "buffer_load_dwordx4 %[vx0], %[srdx], %[tmp] offen lds\n"
        "buffer_load_dwordx4 %[vx1], %[srdx], %[tmp] offen offset:1024 lds\n"
        "buffer_load_dwordx4 %[vx2], %[srdx], %[tmp] offen offset:2048 lds\n"
        "buffer_load_dwordx4 %[vx3], %[srdx], %[tmp] offen offset:3072 lds\n"
        "s_add_u32 m0, %[ring], 16384\n"
        "s_sub_u32 %[tmp], %[sox], 0\n"
        "buffer_load_dwordx4 %[vx0], %[srdx], %[tmp] offen lds\n"
        "buffer_load_dwordx4 %[vx1], %[srdx], %[tmp] offen offset:1024 lds\n"
        "buffer_load_dwordx4 %[vx2], %[srdx], %[tmp] offen offset:2048 lds\n"
        "buffer_load_dwordx4 %[vx3], %[srdx], %[tmp] offen offset:3072 lds\n"
        "s_add_u32 %[sox], %[sox], 131072\n"
        "s_cmp_eq_u32 %[sox], %[soxe]\n"
        "s_cselect_b32 %[sox], %[soxs], %[sox]\n"
        "s_add_u32 m0, %[ring], 4294905856\n"
        "s_sub_u32 %[tmp], %[sox], 4325376\n"
        "buffer_load_dwordx4 %[vx0], %[srdx], %[tmp] offen lds\n"
        "buffer_load_dwordx4 %[vx1], %[srdx], %[tmp] offen offset:1024 lds\n"
        "buffer_load_dwordx4 %[vx2], %[srdx], %[tmp] offen offset:2048 lds\n"
        "buffer_load_dwordx4 %[vx3], %[srdx], %[tmp] offen offset:3072 lds\n"
        "s_add_u32 m0, %[ring], 4294926336\n"
        "s_sub_u32 %[tmp], %[sox], 2883584\n"
        "buffer_load_dwordx4 %[vx0], %[srdx], %[tmp] offen lds\n"
        "buffer_load_dwordx4 %[vx1], %[srdx], %[tmp] offen offset:1024 lds\n"
        "buffer_load_dwordx4 %[vx2], %[srdx], %[tmp] offen offset:2048 lds\n"
        "buffer_load_dwordx4 %[vx3], %[srdx], %[tmp] offen offset:3072 lds\n"
        "s_add_u32 m0, %[ring], 4294946816\n"
        "s_sub_u32 %[tmp], %[sox], 1441792\n"
        "buffer_load_dwordx4 %[vx0], %[srdx], %[tmp] offen lds\n"
        "buffer_load_dwordx4 %[vx1], %[srdx], %[tmp] offen offset:1024 lds\n"
        "buffer_load_dwordx4 %[vx2], %[srdx], %[tmp] offen offset:2048 lds\n"
        "buffer_load_dwordx4 %[vx3], %[srdx], %[tmp] offen offset:3072 lds\n"
        "s_add_u32 m0, %[ring], 0\n"
        "s_sub_u32 %[tmp], %[sox], 0\n"
        "buffer_load_dwordx4 %[vx0], %[srdx], %[tmp] offen lds\n"
        "buffer_load_dwordx4 %[vx1], %[srdx], %[tmp] offen offset:1024 lds\n"
        "buffer_load_dwordx4 %[vx2], %[srdx], %[tmp] offen offset:2048 lds\n"
        "buffer_load_dwordx4 %[vx3], %[srdx], %[tmp] offen offset:3072 lds\n"
        "s_add_u32 %[sox], %[sox], 131072\n"
        "s_cmp_eq_u32 %[sox], %[soxe]\n"
        "s_cselect_b32 %[sox], %[soxs], %[sox]\n"
        "s_add_u32 m0, %[ring], 4294909952\n"
        "s_sub_u32 %[tmp], %[sox], 4325376\n"
        "buffer_load_dwordx4 %[vx0], %[srdx], %[tmp] offen lds\n"
        "buffer_load_dwordx4 %[vx1], %[srdx], %[tmp] offen offset:1024 lds\n"
        "buffer_load_dwordx4 %[vx2], %[srdx], %[tmp] offen offset:2048 lds\n"
        "buffer_load_dwordx4 %[vx3], %[srdx], %[tmp] offen offset:3072 lds\n"
        "s_add_u32 m0, %[ring], 4294930432\n"
        "s_sub_u32 %[tmp], %[sox], 2883584\n"
        "buffer_load_dwordx4 %[vx0], %[srdx], %[tmp] offen lds\n"
        "buffer_load_dwordx4 %[vx1], %[srdx], %[tmp] offen offset:1024 lds\n"
        "buffer_load_dwordx4 %[vx2], %[srdx], %[tmp] offen offset:2048 lds\n"
        "buffer_load_dwordx4 %[vx3], %[srdx], %[tmp] offen offset:3072 lds\n"
        "s_add_u32 m0, %[ring], 4294950912\n"
        "s_sub_u32 %[tmp], %[sox], 1441792\n"
        "buffer_load_dwordx4 %[vx0], %[srdx], %[tmp] offen lds\n"
        "buffer_load_dwordx4 %[vx1], %[srdx], %[tmp] offen offset:1024 lds\n"
        "buffer_load_dwordx4 %[vx2], %[srdx], %[tmp] offen offset:2048 lds\n"
        "buffer_load_dwordx4 %[vx3], %[srdx], %[tmp] offen offset:3072 lds\n"
        "s_add_u32 m0, %[ring], 4096\n"
        "s_sub_u32 %[tmp], %[sox], 0\n"
        "buffer_load_dwordx4 %[vx0], %[srdx], %[tmp] offen lds\n"
        "buffer_load_dwordx4 %[vx1], %[srdx], %[tmp] offen offset:1024 lds\n"
        "buffer_load_dwordx4 %[vx2], %[srdx], %[tmp] offen offset:2048 lds\n"
        "buffer_load_dwordx4 %[vx3], %[srdx], %[tmp] offen offset:3072 lds\n"
        "s_add_u32 %[sox], %[sox], 131072\n"
        "s_cmp_eq_u32 %[sox], %[soxe]\n"
        "s_cselect_b32 %[sox], %[soxs], %[sox]\n"
        "s_waitcnt vmcnt(48)\n"
        "s_branch .Lr3%=\n"
        ".Lo0%=:\n"
        "s_waitcnt lgkmcnt(0)\n"
        "s_barrier\n"
        "ds_read_b128 v[80:83], %[xr] offset:36864\n"
        "ds_read_b128 v[84:87], %[xr] offset:37888\n"
        "ds_read_b128 v[88:91], %[xr] offset:38912\n"
        "ds_read_b128 v[92:95], %[xr] offset:39936\n"
        "ds_read_b128 v[96:99], %[xr] offset:40960\n"
        "ds_read_b128 v[100:103], %[xr] offset:41984\n"
        "ds_read_b128 v[104:107], %[xr] offset:43008\n"
        "ds_read_b128 v[108:111], %[xr] offset:44032\n"
        "ds_read_b128 v[224:227], %[xr] offset:45056\n"
        "ds_read_b128 v[228:231], %[xr] offset:46080\n"
        "ds_read_b128 v[232:235], %[xr] offset:47104\n"
        "ds_read_b128 v[236:239], %[xr] offset:48128\n"
        "s_add_u32 %[sox], %[sox], 393216\n"
        "s_cmp_ge_u32 %[sox], %[soxe]\n"
        "s_cselect_b32 %[tmp], 1441792, 0\n"
        "s_sub_u32 %[sox], %[sox], %[tmp]\n"
        "s_waitcnt vmcnt(0)\n"
        "s_mov_b32 %[tmp], 0x100000\n"
        ".Lx0%=:\n"
        "v_xor_b32 v240, %[tb0], v128\n"
        "v_bitop3_b32 v240, v129, %[tb1], v240 bitop3:0xbe\n"
        "v_bitop3_b32 v240, v130, %[tb2], v240 bitop3:0xbe\n"
        "v_bitop3_b32 v240, v131, %[tb3], v240 bitop3:0xbe\n"
        "v_bitop3_b32 v240, v132, %[tb0], v240 bitop3:0xbe\n"
        "v_bitop3_b32 v240, v133, %[tb1], v240 bitop3:0xbe\n"
        "v_bitop3_b32 v240, v134, %[tb2], v240 bitop3:0xbe\n"
        "v_bitop3_b32 v240, v135, %[tb3], v240 bitop3:0xbe\n"
        "v_bitop3_b32 v240, v136, %[tb0], v240 bitop3:0xbe\n"
        "v_bitop3_b32 v240, v137, %[tb1], v240 bitop3:0xbe\n"
        "v_bitop3_b32 v240, v138, %[tb2], v240 bitop3:0xbe\n"
        "v_bitop3_b32 v240, v139, %[tb3], v240 bitop3:0xbe\n"
        "v_bitop3_b32 v240, v140, %[tb0], v240 bitop3:0xbe\n"
        "v_bitop3_b32 v240, v141, %[tb1], v240 bitop3:0xbe\n"
        "v_bitop3_b32 v240, v142, %[tb2], v240 bitop3:0xbe\n"
        "v_bitop3_b32 v240, v143, %[tb3], v240 bitop3:0xbe\n"
        "v_and_b32 v240, 0xff, v240\n"
        "v_cmp_ne_u32 vcc, 0, v240\n"
        "s_cbranch_vccz .Lx1%=\n"
        "buffer_load_dwordx4 v[128:131], %[vl16], %[srdr], 0 offen offset:0 sc1\n"
        "buffer_load_dwordx4 v[132:135], %[vl16], %[srdr], 0 offen offset:1024 sc1\n"
        "buffer_load_dwordx4 v[136:139], %[vl16], %[srdr], 0 offen offset:2048 sc1\n"
        "buffer_load_dwordx4 v[140:143], %[vl16], %[srdr], 0 offen offset:3072 sc1\n"
        "s_sub_u32 %[tmp], %[tmp], 1\n"
        "s_waitcnt vmcnt(0)\n"
        "s_cbranch_scc0 .Lx0%=\n"
        ".Lx1%=:\n"
        "v_and_b32 v128, v250, v128\n"
        "v_and_b32 v129, v250, v129\n"
        "v_and_b32 v130, v250, v130\n"
        "v_and_b32 v131, v250, v131\n"
        "v_and_b32 v132, v250, v132\n"
        "v_and_b32 v133, v250, v133\n"
        "v_and_b32 v134, v250, v134\n"
        "v_and_b32 v135, v250, v135\n"
        "v_and_b32 v136, v250, v136\n"
        "v_and_b32 v137, v250, v137\n"
        "v_and_b32 v138, v250, v138\n"
        "v_and_b32 v139, v250, v139\n"
        "v_and_b32 v140, v250, v140\n"
        "v_and_b32 v141, v250, v141\n"
        "v_and_b32 v142, v250, v142\n"
        "v_and_b32 v143, v250, v143\n"
        "v_pk_add_f32 v[64:65], v[64:65], v[128:129]\n"
        "v_pk_add_f32 v[66:67], v[66:67], v[130:131]\n"
        "v_pk_add_f32 v[68:69], v[68:69], v[132:133]\n"
        "v_pk_add_f32 v[70:71], v[70:71], v[134:135]\n"
        "v_pk_add_f32 v[72:73], v[72:73], v[136:137]\n"
        "v_pk_add_f32 v[74:75], v[74:75], v[138:139]\n"
        "v_pk_add_f32 v[76:77], v[76:77], v[140:141]\n"
        "v_pk_add_f32 v[78:79], v[78:79], v[142:143]\n"
        "s_waitcnt lgkmcnt(8)\n"
        "v_pk_add_f32 v[64:65], v[64:65], v[80:81]\n"
        "v_pk_add_f32 v[66:67], v[66:67], v[82:83]\n"
        "v_pk_add_f32 v[68:69], v[68:69], v[84:85]\n"
        "v_pk_add_f32 v[70:71], v[70:71], v[86:87]\n"
        "v_pk_add_f32 v[72:73], v[72:73], v[88:89]\n"
        "v_pk_add_f32 v[74:75], v[74:75], v[90:91]\n"
        "v_pk_add_f32 v[76:77], v[76:77], v[92:93]\n"
        "v_pk_add_f32 v[78:79], v[78:79], v[94:95]\n"
        "s_waitcnt lgkmcnt(4)\n"
        "v_pk_add_f32 v[64:65], v[64:65], v[96:97]\n"
        "v_pk_add_f32 v[66:67], v[66:67], v[98:99]\n"
        "v_pk_add_f32 v[68:69], v[68:69], v[100:101]\n"
        "v_pk_add_f32 v[70:71], v[70:71], v[102:103]\n"
        "v_pk_add_f32 v[72:73], v[72:73], v[104:105]\n"
        "v_pk_add_f32 v[74:75], v[74:75], v[106:107]\n"
        "v_pk_add_f32 v[76:77], v[76:77], v[108:109]\n"
        "v_pk_add_f32 v[78:79], v[78:79], v[110:111]\n"
        "s_waitcnt lgkmcnt(0)\n"
        "v_pk_add_f32 v[64:65], v[64:65], v[224:225]\n"
        "v_pk_add_f32 v[66:67], v[66:67], v[226:227]\n"
        "v_pk_add_f32 v[68:69], v[68:69], v[228:229]\n"
        "v_pk_add_f32 v[70:71], v[70:71], v[230:231]\n"
        "v_pk_add_f32 v[72:73], v[72:73], v[232:233]\n"
        "v_pk_add_f32 v[74:75], v[74:75], v[234:235]\n"
        "v_pk_add_f32 v[76:77], v[76:77], v[236:237]\n"
        "v_pk_add_f32 v[78:79], v[78:79], v[238:239]\n"
        ".Lr3%=:\n"
        : [sow] "+s"(sow), [soa] "+s"(soa), [sox] "+s"(sox), [tmp] "=&s"(tmp),
          "=&{v[64:79]}"(acc), [b2v] "=&v"(b2v)
        : W2_IN, [vw0] "v"(vw0), [vw1] "v"(vw1), [vw2] "v"(vw2), [srdw] "s"(srdw), [sowe] "s"(sowe), [sows] "s"(sows),
          [tr0] "v"(tr0), [tr1] "v"(tr1), [tr2] "v"(tr2), [vhk] "v"(vhk), [vhs] "v"(vhs), [srda] "s"(srda), [soae] "s"(soae), [soas] "s"(soas),
          [vfl] "v"(vfl), [vf2] "v"(WS_FL2 + (lane & 15) * 128 + own * 4), [tag] "s"(tag), [vbias] "v"(hh * 8), [srdb] "s"(srdb), [vb2] "v"(vb2), [srd2] "s"(srd2),
          [xa0] "v"(xa0), [xa1] "v"(xa1), [xa2] "v"(xa2), [xr] "v"(xr), [wv] "s"(w), [ph] "s"((j >> 2) & 1), [vl16] "v"(vl16), [srds] "s"(srds), [srdr] "s"(srdr),
          [tb0] "s"(tag & 255), [tb1] "s"((tag >> 8) & 255), [tb2] "s"((tag >> 16) & 255), [tb3] "s"(tag >> 24)
        : "memory", "scc", "vcc", VCLOB_ALL, ACLOB_ALL);
    STAMP(7);
    if (w != 3) {
    u32x4 o[2];
#pragma unroll
    for (int r = 0; r < 8; ++r) {
      const f32x2 u = {acc[2 * r], acc[2 * r + 1]};
      constexpr float k0 = -2.0f * 0.7978845608028654f * 1.4426950408889634f, k1 = k0 * 0.044715f;
      const f32x2 t = u * (u * u * k1 + k0);
      const f32x2 dn = f32x2{__builtin_amdgcn_exp2f(t[0]), __builtin_amdgcn_exp2f(t[1])} + 1.0f;
      const f32x2 g = u * f32x2{__builtin_amdgcn_rcpf(dn[0]), __builtin_amdgcn_rcpf(dn[1])};
      o[r >> 2][r & 3] = min(pk_bf16(g[0], g[1]), 0xfffffffeu);
    }
    asm volatile(
        "buffer_store_dwordx4 %[o0], %[vl16], %[srdg], 0 offen sc1\n"
        "buffer_store_dwordx4 %[o1], %[vl16], %[srdg], 0 offen offset:2048 sc1\n"
        :: [o0] "v"(o[0]), [o1] "v"(o[1]), [vl16] "v"(vl16), [srdg] "s"(srdg) : "memory");
    }
    STAMP(8);
    asm volatile(
        "s_add_u32 m0, %[ring], 8192\n"
        "s_add_u32 %[tmp], %[sox], 131072\n"
        "buffer_load_dwordx4 %[vx0], %[srdx], %[sox] offen lds\n"
        "buffer_load_dwordx4 %[vx1], %[srdx], %[sox] offen offset:1024 lds\n"
        "buffer_load_dwordx4 %[vx2], %[srdx], %[sox] offen offset:2048 lds\n"
        "buffer_load_dwordx4 %[vx3], %[srdx], %[sox] offen offset:3072 lds\n"
        "s_cmp_eq_u32 %[tmp], %[soxe]\n"
        "s_cselect_b32 %[sox], %[soxs], %[tmp]\n"
        "s_add_u32 m0, %[ring], 12288\n"
        "s_add_u32 %[tmp], %[sox], 131072\n"
        "buffer_load_dwordx4 %[vx0], %[srdx], %[sox] offen lds\n"
        "buffer_load_dwordx4 %[vx1], %[srdx], %[sox] offen offset:1024 lds\n"
        "buffer_load_dwordx4 %[vx2], %[srdx], %[sox] offen offset:2048 lds\n"
        "buffer_load_dwordx4 %[vx3], %[srdx], %[sox] offen offset:3072 lds\n"
        "s_cmp_eq_u32 %[tmp], %[soxe]\n"
        "s_cselect_b32 %[sox], %[soxs], %[tmp]\n"
        "s_cmp_lg_u32 %[wv], 3\n"
        "s_cbranch_scc1 .Lw%=\n"
        "s_waitcnt vmcnt(8)\n"
        ".Lw%=:\n"
        "s_barrier\n"
        "ds_read_b64_tr_b16 a[240:241], %[tq0] offset:16384\n"
        "ds_read_b64_tr_b16 a[242:243], %[tq0] offset:17408\n"
        "ds_read_b64_tr_b16 a[244:245], %[tq1] offset:16384\n"
        "ds_read_b64_tr_b16 a[246:247], %[tq1] offset:17408\n"
        "ds_read_b64_tr_b16 a[248:249], %[tq0] offset:18432\n"
        "ds_read_b64_tr_b16 a[250:251], %[tq0] offset:19456\n"
        "ds_read_b64_tr_b16 a[252:253], %[tq1] offset:18432\n"
        "ds_read_b64_tr_b16 a[254:255], %[tq1] offset:19456\n"
        "s_waitcnt lgkmcnt(7)\n"
        "ds_read_b64_tr_b16 a[0:1], %[tq0] offset:0\n"
        "ds_read_b64_tr_b16 a[2:3], %[tq0] offset:1024\n"
        "ds_read_b64_tr_b16 a[4:5], %[tq1] offset:0\n"
        "ds_read_b64_tr_b16 a[6:7], %[tq1] offset:1024\n"
        "ds_read_b64_tr_b16 a[8:9], %[tq0] offset:2048\n"
        "ds_read_b64_tr_b16 a[10:11], %[tq0] offset:3072\n"
        "ds_read_b64_tr_b16 a[12:13], %[tq1] offset:2048\n"
        "ds_read_b64_tr_b16 a[14:15], %[tq1] offset:3072\n"
        "s_waitcnt lgkmcnt(8)\n"
        "s_add_u32 m0, %[ring], 16384\n"
        "s_add_u32 %[tmp], %[sox], 131072\n"
        "buffer_load_dwordx4 %[vx0], %[srdx], %[sox] offen lds\n"
        "buffer_load_dwordx4 %[vx1], %[srdx], %[sox] offen offset:1024 lds\n"
        "buffer_load_dwordx4 %[vx2], %[srdx], %[sox] offen offset:2048 lds\n"
        "buffer_load_dwordx4 %[vx3], %[srdx], %[sox] offen offset:3072 lds\n"
        "s_cmp_eq_u32 %[tmp], %[soxe]\n"
        "s_cselect_b32 %[sox], %[soxs], %[tmp]\n"
        "s_waitcnt lgkmcnt(0)\n"
        "s_add_u32 m0, %[ring], 0\n"
        "s_add_u32 %[tmp], %[sox], 131072\n"
        "buffer_load_dwordx4 %[vx0], %[srdx], %[sox] offen lds\n"
        "buffer_load_dwordx4 %[vx1], %[srdx], %[sox] offen offset:1024 lds\n"
        "buffer_load_dwordx4 %[vx2], %[srdx], %[sox] offen offset:2048 lds\n"
        "buffer_load_dwordx4 %[vx3], %[srdx], %[sox] offen offset:3072 lds\n"
        "s_cmp_eq_u32 %[tmp], %[soxe]\n"
        "s_cselect_b32 %[sox], %[soxs], %[tmp]\n"
        "s_add_u32 m0, %[ringy], 0\n"
        "s_add_u32 %[tmp], %[sox], 131072\n"
        "buffer_load_dwordx4 %[vx0], %[srdx], %[sox] offen lds\n"
        "buffer_load_dwordx4 %[vx1], %[srdx], %[sox] offen offset:1024 lds\n"
        "buffer_load_dwordx4 %[vx2], %[srdx], %[sox] offen offset:2048 lds\n"
        "buffer_load_dwordx4 %[vx3], %[srdx], %[sox] offen offset:3072 lds\n"
        "s_cmp_eq_u32 %[tmp], %[soxe]\n"
        "s_cselect_b32 %[sox], %[soxs], %[tmp]\n"
        : [sox] "+s"(sox), [tmp] "=&s"(tmp) : W2_IN, [wv] "s"(w) : "memory", "scc", ACLOB_ALL);
  } else {
    const unsigned si = (j & 7) * 2 + half;
    const uint64_t pg = wsd + WS_G + (si * 4 + w) * 22016u;
    const i32x4 srdp = make_srd(pg, 22016), srdq = make_srd(pg + 20480, 1536);
    asm volatile(
        "s_waitcnt vmcnt(0)\n"
        "buffer_store_dword %[vtag], %[vfo], %[srdc], 0 offen sc1\n"
        "s_mov_b32 %[tmp], 0\n"
        "buffer_store_dwordx4 %[ones], %[vl16], %[srdp], %[tmp] offen offset:0 sc1\n"
        "buffer_store_dwordx4 %[ones], %[vl16], %[srdp], %[tmp] offen offset:1024 sc1\n"
        "buffer_store_dwordx4 %[ones], %[vl16], %[srdp], %[tmp] offen offset:2048 sc1\n"
        "buffer_store_dwordx4 %[ones], %[vl16], %[srdp], %[tmp] offen offset:3072 sc1\n"
        "s_mov_b32 %[tmp], 4096\n"
        "buffer_store_dwordx4 %[ones], %[vl16], %[srdp], %[tmp] offen offset:0 sc1\n"
        "buffer_store_dwordx4 %[ones], %[vl16], %[srdp], %[tmp] offen offset:1024 sc1\n"
        "buffer_store_dwordx4 %[ones], %[vl16], %[srdp], %[tmp] offen offset:2048 sc1\n"
        "buffer_store_dwordx4 %[ones], %[vl16], %[srdp], %[tmp] offen offset:3072 sc1\n"
        "s_mov_b32 %[tmp], 8192\n"
        "buffer_store_dwordx4 %[ones], %[vl16], %[srdp], %[tmp] offen offset:0 sc1\n"
        "buffer_store_dwordx4 %[ones], %[vl16], %[srdp], %[tmp] offen offset:1024 sc1\n"
        "buffer_store_dwordx4 %[ones], %[vl16], %[srdp], %[tmp] offen offset:2048 sc1\n"
        "buffer_store_dwordx4 %[ones], %[vl16], %[srdp], %[tmp] offen offset:3072 sc1\n"
        "s_mov_b32 %[tmp], 12288\n"
        "buffer_store_dwordx4 %[ones], %[vl16], %[srdp], %[tmp] offen offset:0 sc1\n"
        "buffer_store_dwordx4 %[ones], %[vl16], %[srdp], %[tmp] offen offset:1024 sc1\n"
        "buffer_store_dwordx4 %[ones], %[vl16], %[srdp], %[tmp] offen offset:2048 sc1\n"
        "buffer_store_dwordx4 %[ones], %[vl16], %[srdp], %[tmp] offen offset:3072 sc1\n"
        "s_mov_b32 %[tmp], 16384\n"
        "buffer_store_dwordx4 %[ones], %[vl16], %[srdp], %[tmp] offen offset:0 sc1\n"
        "buffer_store_dwordx4 %[ones], %[vl16], %[srdp], %[tmp] offen offset:1024 sc1\n"
        "buffer_store_dwordx4 %[ones], %[vl16], %[srdp], %[tmp] offen offset:2048 sc1\n"
        "buffer_store_dwordx4 %[ones], %[vl16], %[srdp], %[tmp] offen offset:3072 sc1\n"
        "s_mov_b32 %[tmp], 20480\n"
        "buffer_store_dwordx4 %[ones], %[vl16], %[srdq], 0 offen offset:0 sc1\n"
        "buffer_store_dwordx4 %[ones], %[vl16], %[srdq], 0 offen offset:1024 sc1\n"
        "s_waitcnt vmcnt(0)\n"
        "buffer_store_dword %[vtag], %[vf2], %[srdc], 0 offen sc1\n"
        "buffer_load_ushort %[b2v], %[vb2], %[srd2], 0 offen\n"
        "s_add_u32 m0, %[ringx], 0\n"
        "s_add_u32 %[tmp], %[sox], 131072\n"
        "buffer_load_dwordx4 %[vx0], %[srdx], %[sox] offen lds\n"
        "buffer_load_dwordx4 %[vx1], %[srdx], %[sox] offen offset:1024 lds\n"
        "buffer_load_dwordx4 %[vx2], %[srdx], %[sox] offen offset:2048 lds\n"
        "buffer_load_dwordx4 %[vx3], %[srdx], %[sox] offen offset:3072 lds\n"
        "s_cmp_eq_u32 %[tmp], %[soxe]\n"
        "s_cselect_b32 %[sox], %[soxs], %[tmp]\n"
        "s_add_u32 m0, %[ringx], 4096\n"
        "s_add_u32 %[tmp], %[sox], 131072\n"
        "buffer_load_dwordx4 %[vx0], %[srdx], %[sox] offen lds\n"
        "buffer_load_dwordx4 %[vx1], %[srdx], %[sox] offen offset:1024 lds\n"
        "buffer_load_dwordx4 %[vx2], %[srdx], %[sox] offen offset:2048 lds\n"
        "buffer_load_dwordx4 %[vx3], %[srdx], %[sox] offen offset:3072 lds\n"
        "s_cmp_eq_u32 %[tmp], %[soxe]\n"
        "s_cselect_b32 %[sox], %[soxs], %[tmp]\n"
        "s_add_u32 m0, %[ring], 12288\n"
        "s_add_u32 %[tmp], %[sox], 131072\n"
        "buffer_load_dwordx4 %[vx0], %[srdx], %[sox] offen lds\n"
        "buffer_load_dwordx4 %[vx1], %[srdx], %[sox] offen offset:1024 lds\n"
        "buffer_load_dwordx4 %[vx2], %[srdx], %[sox] offen offset:2048 lds\n"
        "buffer_load_dwordx4 %[vx3], %[srdx], %[sox] offen offset:3072 lds\n"
        "s_cmp_eq_u32 %[tmp], %[soxe]\n"
        "s_cselect_b32 %[sox], %[soxs], %[tmp]\n"
        "s_add_u32 m0, %[ring], 16384\n"
        "s_add_u32 %[tmp], %[sox], 131072\n"
        "buffer_load_dwordx4 %[vx0], %[srdx], %[sox] offen lds\n"
        "buffer_load_dwordx4 %[vx1], %[srdx], %[sox] offen offset:1024 lds\n"
        "buffer_load_dwordx4 %[vx2], %[srdx], %[sox] offen offset:2048 lds\n"
        "buffer_load_dwordx4 %[vx3], %[srdx], %[sox] offen offset:3072 lds\n"
        "s_cmp_eq_u32 %[tmp], %[soxe]\n"
        "s_cselect_b32 %[sox], %[soxs], %[tmp]\n"
        "s_add_u32 m0, %[ring], 0\n"
        "s_add_u32 %[tmp], %[sox], 131072\n"
        "buffer_load_dwordx4 %[vx0], %[srdx], %[sox] offen lds\n"
        "buffer_load_dwordx4 %[vx1], %[srdx], %[sox] offen offset:1024 lds\n"
        "buffer_load_dwordx4 %[vx2], %[srdx], %[sox] offen offset:2048 lds\n"
        "buffer_load_dwordx4 %[vx3], %[srdx], %[sox] offen offset:3072 lds\n"
        "s_cmp_eq_u32 %[tmp], %[soxe]\n"
        "s_cselect_b32 %[sox], %[soxs], %[tmp]\n"
        "s_add_u32 m0, %[ring], 4096\n"
        "s_add_u32 %[tmp], %[sox], 131072\n"
        "buffer_load_dwordx4 %[vx0], %[srdx], %[sox] offen lds\n"
        "buffer_load_dwordx4 %[vx1], %[srdx], %[sox] offen offset:1024 lds\n"
        "buffer_load_dwordx4 %[vx2], %[srdx], %[sox] offen offset:2048 lds\n"
        "buffer_load_dwordx4 %[vx3], %[srdx], %[sox] offen offset:3072 lds\n"
        "s_cmp_eq_u32 %[tmp], %[soxe]\n"
        "s_cselect_b32 %[sox], %[soxs], %[tmp]\n"
        "s_add_u32 m0, %[ring], 8192\n"
        "s_add_u32 %[tmp], %[sox], 131072\n"
        "buffer_load_dwordx4 %[vx0], %[srdx], %[sox] offen lds\n"
        "buffer_load_dwordx4 %[vx1], %[srdx], %[sox] offen offset:1024 lds\n"
        "buffer_load_dwordx4 %[vx2], %[srdx], %[sox] offen offset:2048 lds\n"
        "buffer_load_dwordx4 %[vx3], %[srdx], %[sox] offen offset:3072 lds\n"
        "s_cmp_eq_u32 %[tmp], %[soxe]\n"
        "s_cselect_b32 %[sox], %[soxs], %[tmp]\n"
        "s_waitcnt vmcnt(24)\n"
        "ds_read_b64_tr_b16 a[192:193], %[tqx0] offset:0\n"
        "ds_read_b64_tr_b16 a[194:195], %[tqx0] offset:1024\n"
        "ds_read_b64_tr_b16 a[196:197], %[tqx1] offset:0\n"
        "ds_read_b64_tr_b16 a[198:199], %[tqx1] offset:1024\n"
        "ds_read_b64_tr_b16 a[200:201], %[tqx0] offset:2048\n"
        "ds_read_b64_tr_b16 a[202:203], %[tqx0] offset:3072\n"
        "ds_read_b64_tr_b16 a[204:205], %[tqx1] offset:2048\n"
        "ds_read_b64_tr_b16 a[206:207], %[tqx1] offset:3072\n"
        "s_waitcnt vmcnt(20) lgkmcnt(7)\n"
        "ds_read_b64_tr_b16 a[208:209], %[tqx0] offset:4096\n"
        "ds_read_b64_tr_b16 a[210:211], %[tqx0] offset:5120\n"
        "ds_read_b64_tr_b16 a[212:213], %[tqx1] offset:4096\n"
        "ds_read_b64_tr_b16 a[214:215], %[tqx1] offset:5120\n"
        "ds_read_b64_tr_b16 a[216:217], %[tqx0] offset:6144\n"
        "ds_read_b64_tr_b16 a[218:219], %[tqx0] offset:7168\n"
        "ds_read_b64_tr_b16 a[220:221], %[tqx1] offset:6144\n"
        "ds_read_b64_tr_b16 a[222:223], %[tqx1] offset:7168\n"
        "s_waitcnt vmcnt(16) lgkmcnt(7)\n"
        "ds_read_b64_tr_b16 a[224:225], %[tq0] offset:12288\n"
        "ds_read_b64_tr_b16 a[226:227], %[tq0] offset:13312\n"
        "ds_read_b64_tr_b16 a[228:229], %[tq1] offset:12288\n"
        "ds_read_b64_tr_b16 a[230:231], %[tq1] offset:13312\n"
        "ds_read_b64_tr_b16 a[232:233], %[tq0] offset:14336\n"
        "ds_read_b64_tr_b16 a[234:235], %[tq0] offset:15360\n"
        "ds_read_b64_tr_b16 a[236:237], %[tq1] offset:14336\n"
        "ds_read_b64_tr_b16 a[238:239], %[tq1] offset:15360\n"
        "s_waitcnt lgkmcnt(0)\n"
        "s_add_u32 m0, %[ring], 12288\n"
        "s_add_u32 %[tmp], %[sox], 131072\n"
        "buffer_load_dwordx4 %[vx0], %[srdx], %[sox] offen lds\n"
        "buffer_load_dwordx4 %[vx1], %[srdx], %[sox] offen offset:1024 lds\n"
        "buffer_load_dwordx4 %[vx2], %[srdx], %[sox] offen offset:2048 lds\n"
        "buffer_load_dwordx4 %[vx3], %[srdx], %[sox] offen offset:3072 lds\n"
        "s_cmp_eq_u32 %[tmp], %[soxe]\n"
        "s_cselect_b32 %[sox], %[soxs], %[tmp]\n"
        "s_waitcnt vmcnt(16)\n"
        "ds_read_b64_tr_b16 a[240:241], %[tq0] offset:16384\n"
        "ds_read_b64_tr_b16 a[242:243], %[tq0] offset:17408\n"
        "ds_read_b64_tr_b16 a[244:245], %[tq1] offset:16384\n"
        "ds_read_b64_tr_b16 a[246:247], %[tq1] offset:17408\n"
        "ds_read_b64_tr_b16 a[248:249], %[tq0] offset:18432\n"
        "ds_read_b64_tr_b16 a[250:251], %[tq0] offset:19456\n"
        "ds_read_b64_tr_b16 a[252:253], %[tq1] offset:18432\n"
        "ds_read_b64_tr_b16 a[254:255], %[tq1] offset:19456\n"
        "s_waitcnt lgkmcnt(0)\n"
        "s_add_u32 m0, %[ring], 16384\n"
        "s_add_u32 %[tmp], %[sox], 131072\n"
        "buffer_load_dwordx4 %[vx0], %[srdx], %[sox] offen lds\n"
        "buffer_load_dwordx4 %[vx1], %[srdx], %[sox] offen offset:1024 lds\n"
        "buffer_load_dwordx4 %[vx2], %[srdx], %[sox] offen offset:2048 lds\n"
        "buffer_load_dwordx4 %[vx3], %[srdx], %[sox] offen offset:3072 lds\n"
        "s_cmp_eq_u32 %[tmp], %[soxe]\n"
        "s_cselect_b32 %[sox], %[soxs], %[tmp]\n"
        "s_waitcnt vmcnt(16)\n"
        "ds_read_b64_tr_b16 a[0:1], %[tq0] offset:0\n"
        "ds_read_b64_tr_b16 a[2:3], %[tq0] offset:1024\n"
        "ds_read_b64_tr_b16 a[4:5], %[tq1] offset:0\n"
        "ds_read_b64_tr_b16 a[6:7], %[tq1] offset:1024\n"
        "ds_read_b64_tr_b16 a[8:9], %[tq0] offset:2048\n"
        "ds_read_b64_tr_b16 a[10:11], %[tq0] offset:3072\n"
        "ds_read_b64_tr_b16 a[12:13], %[tq1] offset:2048\n"
        "ds_read_b64_tr_b16 a[14:15], %[tq1] offset:3072\n"
        "s_waitcnt lgkmcnt(0)\n"
        "s_add_u32 m0, %[ring], 0\n"
        "s_add_u32 %[tmp], %[sox], 131072\n"
        "buffer_load_dwordx4 %[vx0], %[srdx], %[sox] offen lds\n"
        "buffer_load_dwordx4 %[vx1], %[srdx], %[sox] offen offset:1024 lds\n"
        "buffer_load_dwordx4 %[vx2], %[srdx], %[sox] offen offset:2048 lds\n"
        "buffer_load_dwordx4 %[vx3], %[srdx], %[sox] offen offset:3072 lds\n"
        "s_cmp_eq_u32 %[tmp], %[soxe]\n"
        "s_cselect_b32 %[sox], %[soxs], %[tmp]\n"
        "s_add_u32 m0, %[ringy], 0\n"
        "s_add_u32 %[tmp], %[sox], 131072\n"
        "buffer_load_dwordx4 %[vx0], %[srdx], %[sox] offen lds\n"
        "buffer_load_dwordx4 %[vx1], %[srdx], %[sox] offen offset:1024 lds\n"
        "buffer_load_dwordx4 %[vx2], %[srdx], %[sox] offen offset:2048 lds\n"
        "buffer_load_dwordx4 %[vx3], %[srdx], %[sox] offen offset:3072 lds\n"
        "s_cmp_eq_u32 %[tmp], %[soxe]\n"
        "s_cselect_b32 %[sox], %[soxs], %[tmp]\n"
        "s_waitcnt vmcnt(0)\n"
        : [sox] "+s"(sox), [tmp] "=&s"(tmp), [b2v] "=&v"(b2v)
        : W2_IN, [vb2] "v"(vb2), [srd2] "s"(srd2), [ones] "v"(u32x4{~0u, ~0u, ~0u, ~0u}), [vl16] "v"(vl16), [srdp] "s"(srdp), [srdq] "s"(srdq),
          [vf2] "v"(lane ? (unsigned)WS_CTRL : WS_FL2 + si * 128u + w * 4u)
        : "memory", "scc", ACLOB_ALL);
    for (unsigned it = 0; it < SPIN_CAP; ++it) {
      const u32x4 fl = __builtin_amdgcn_raw_buffer_load_b128(rsc, WS_FL + threadIdx.x * 128, 0, 16);
      const u32x4 fm = (fl ^ tag) | (__builtin_amdgcn_raw_buffer_load_b128(rsc, WS_FL2 + (lane & 15) * 128, 0, 16) ^ tag);
      const unsigned f0_ = fm[0], f1_ = fm[1], f2_ = fm[2], f3_ = fm[3];
      if (__all((f0_ | f1_ | f2_ | f3_) == 0)) break;
      __builtin_amdgcn_s_sleep(8);
    }
    __builtin_amdgcn_s_barrier();
    if (b == 31 * 8 + 2 * LIGHT && threadIdx.x == 0) __builtin_amdgcn_raw_buffer_store_b32(tag, rsc, WS_E, 0, 16);
    STAMP(8);
  }
  {
    unsigned so1, so2, so3, bad;
    unsigned long long ret;
    f32x16 acc;
    asm volatile(
        "buffer_load_dwordx4 v[160:163], %[va], %[srda], %[so0] offen sc1\n"
        "buffer_load_dwordx4 v[164:167], %[vb], %[srda], %[so0] offen sc1\n"
        "buffer_load_dwordx4 v[168:171], %[va], %[srda], %[so0] offen offset:2048 sc1\n"
        "buffer_load_dwordx4 v[172:175], %[vb], %[srda], %[so0] offen offset:2048 sc1\n"
        "v_lshlrev_b32 v64, 16, %[b2v]\n"
        "v_lshlrev_b32 v65, 16, %[b2v]\n"
        "v_lshlrev_b32 v66, 16, %[b2v]\n"
        "v_lshlrev_b32 v67, 16, %[b2v]\n"
        "v_lshlrev_b32 v68, 16, %[b2v]\n"
        "v_lshlrev_b32 v69, 16, %[b2v]\n"
        "v_lshlrev_b32 v70, 16, %[b2v]\n"
        "v_lshlrev_b32 v71, 16, %[b2v]\n"
        "v_lshlrev_b32 v72, 16, %[b2v]\n"
        "v_lshlrev_b32 v73, 16, %[b2v]\n"
        "v_lshlrev_b32 v74, 16, %[b2v]\n"
        "v_lshlrev_b32 v75, 16, %[b2v]\n"
        "v_lshlrev_b32 v76, 16, %[b2v]\n"
        "v_lshlrev_b32 v77, 16, %[b2v]\n"
        "v_lshlrev_b32 v78, 16, %[b2v]\n"
        "v_lshlrev_b32 v79, 16, %[b2v]\n"
        "s_add_u32 %[so1], %[so0], 4096\n"
        "s_cmp_eq_u32 %[so1], %[soae]\n"
        "s_cselect_b32 %[so1], %[soas], %[so1]\n"
        "buffer_load_dwordx4 v[176:179], %[va], %[srda], %[so1] offen sc1\n"
        "buffer_load_dwordx4 v[180:183], %[vb], %[srda], %[so1] offen sc1\n"
        "buffer_load_dwordx4 v[184:187], %[va], %[srda], %[so1] offen offset:2048 sc1\n"
        "buffer_load_dwordx4 v[188:191], %[vb], %[srda], %[so1] offen offset:2048 sc1\n"
        "s_add_u32 %[so2], %[so1], 4096\n"
        "s_cmp_eq_u32 %[so2], %[soae]\n"
        "s_cselect_b32 %[so2], %[soas], %[so2]\n"
        "buffer_load_dwordx4 v[192:195], %[va], %[srda], %[so2] offen sc1\n"
        "buffer_load_dwordx4 v[196:199], %[vb], %[srda], %[so2] offen sc1\n"
        "buffer_load_dwordx4 v[200:203], %[va], %[srda], %[so2] offen offset:2048 sc1\n"
        "buffer_load_dwordx4 v[204:207], %[vb], %[srda], %[so2] offen offset:2048 sc1\n"
        "s_add_u32 %[so3], %[so2], 4096\n"
        "s_cmp_eq_u32 %[so3], %[soae]\n"
        "s_cselect_b32 %[so3], %[soas], %[so3]\n"
        "buffer_load_dwordx4 v[208:211], %[va], %[srda], %[so3] offen sc1\n"
        "buffer_load_dwordx4 v[212:215], %[vb], %[srda], %[so3] offen sc1\n"
        "buffer_load_dwordx4 v[216:219], %[va], %[srda], %[so3] offen offset:2048 sc1\n"
        "buffer_load_dwordx4 v[220:223], %[vb], %[srda], %[so3] offen offset:2048 sc1\n"
        STAMP_ASM(9)
        "s_waitcnt vmcnt(12)\n"
        "v_max_u32 v240, v160, v161\n"
        "v_max3_u32 v240, v162, v163, v240\n"
        "v_max3_u32 v240, v164, v165, v240\n"
        "v_max3_u32 v240, v166, v167, v240\n"
        "v_max3_u32 v240, v168, v169, v240\n"
        "v_max3_u32 v240, v170, v171, v240\n"
        "v_max3_u32 v240, v172, v173, v240\n"
        "v_max3_u32 v240, v174, v175, v240\n"
        "v_cmp_eq_u32 vcc, -1, v240\n"
        "s_cbranch_vccnz .Ls0_%=\n"
        ".Lb0_%=:\n"
        STAMP_ASM(10)
        "v_mfma_f32_32x32x16_bf16 v[64:79], v[160:163], a[192:195], v[64:79]\n"
        "ds_read_b64_tr_b16 a[16:17], %[tq0] offset:4096\n"
        "v_mfma_f32_32x32x16_bf16 v[80:95], v[164:167], a[192:195], 0\n"
        "ds_read_b64_tr_b16 a[18:19], %[tq0] offset:5120\n"
        "s_waitcnt vmcnt(10)\n"
        "v_mfma_f32_32x32x16_bf16 v[96:111], v[160:163], a[196:199], 0\n"
        "ds_read_b64_tr_b16 a[20:21], %[tq1] offset:4096\n"
        "v_max_u32 v240, v176, v177\n"
        "v_max3_u32 v240, v178, v179, v240\n"
        "v_mfma_f32_32x32x16_bf16 v[112:127], v[164:167], a[196:199], 0\n"
        "ds_read_b64_tr_b16 a[22:23], %[tq1] offset:5120\n"
        "v_max3_u32 v240, v180, v181, v240\n"
        "v_max3_u32 v240, v182, v183, v240\n"
        "s_add_u32 %[so0], %[so3], 4096\n"
        "s_cmp_eq_u32 %[so0], %[soae]\n"
        "s_cselect_b32 %[so0], %[soas], %[so0]\n"
        "buffer_load_dwordx4 v[160:163], %[va], %[srda], %[so0] offen sc1\n"
        "buffer_load_dwordx4 v[164:167], %[vb], %[srda], %[so0] offen sc1\n"
        "v_mfma_f32_32x32x16_bf16 v[80:95], v[172:175], a[200:203], v[80:95]\n"
        "ds_read_b64_tr_b16 a[24:25], %[tq0] offset:6144\n"
        "s_waitcnt vmcnt(10)\n"
        "v_mfma_f32_32x32x16_bf16 v[96:111], v[168:171], a[204:207], v[96:111]\n"
        "ds_read_b64_tr_b16 a[26:27], %[tq0] offset:7168\n"
        "v_max3_u32 v240, v184, v185, v240\n"
        "v_max3_u32 v240, v186, v187, v240\n"
        "v_mfma_f32_32x32x16_bf16 v[112:127], v[172:175], a[204:207], v[112:127]\n"
        "ds_read_b64_tr_b16 a[28:29], %[tq1] offset:6144\n"
        "v_max3_u32 v240, v188, v189, v240\n"
        "v_max3_u32 v240, v190, v191, v240\n"
        "v_mfma_f32_32x32x16_bf16 v[64:79], v[168:171], a[200:203], v[64:79]\n"
        "ds_read_b64_tr_b16 a[30:31], %[tq1] offset:7168\n"
        "buffer_load_dwordx4 v[168:171], %[va], %[srda], %[so0] offen offset:2048 sc1\n"
        "buffer_load_dwordx4 v[172:175], %[vb], %[srda], %[so0] offen offset:2048 sc1\n"
        "v_cmp_eq_u32 vcc, -1, v240\n"
        "s_cbranch_vccnz .Ls1_%=\n"
        ".Lb1_%=:\n"
        "v_mfma_f32_32x32x16_bf16 v[64:79], v[176:179], a[208:211], v[64:79]\n"
        "ds_read_b64_tr_b16 a[32:33], %[tq0] offset:8192\n"
        "v_mfma_f32_32x32x16_bf16 v[80:95], v[180:183], a[208:211], v[80:95]\n"
        "ds_read_b64_tr_b16 a[34:35], %[tq0] offset:9216\n"
        "s_waitcnt vmcnt(10)\n"
        "v_mfma_f32_32x32x16_bf16 v[96:111], v[176:179], a[212:215], v[96:111]\n"
        "ds_read_b64_tr_b16 a[36:37], %[tq1] offset:8192\n"
        "v_max_u32 v240, v192, v193\n"
        "v_max3_u32 v240, v194, v195, v240\n"
        "v_mfma_f32_32x32x16_bf16 v[112:127], v[180:183], a[212:215], v[112:127]\n"
        "ds_read_b64_tr_b16 a[38:39], %[tq1] offset:9216\n"
        "v_max3_u32 v240, v196, v197, v240\n"
        "v_max3_u32 v240, v198, v199, v240\n"
        "s_add_u32 %[so1], %[so0], 4096\n"
        "s_cmp_eq_u32 %[so1], %[soae]\n"
        "s_cselect_b32 %[so1], %[soas], %[so1]\n"
        "buffer_load_dwordx4 v[176:179], %[va], %[srda], %[so1] offen sc1\n"
        "buffer_load_dwordx4 v[180:183], %[vb], %[srda], %[so1] offen sc1\n"
        "v_mfma_f32_32x32x16_bf16 v[80:95], v[188:191], a[216:219], v[80:95]\n"
        "ds_read_b64_tr_b16 a[40:41], %[tq0] offset:10240\n"
        "s_waitcnt vmcnt(10)\n"
        "v_mfma_f32_32x32x16_bf16 v[96:111], v[184:187], a[220:223], v[96:111]\n"
        "ds_read_b64_tr_b16 a[42:43], %[tq0] offset:11264\n"
        "v_max3_u32 v240, v200, v201, v240\n"
        "v_max3_u32 v240, v202, v203, v240\n"
        "v_mfma_f32_32x32x16_bf16 v[112:127], v[188:191], a[220:223], v[112:127]\n"
        "ds_read_b64_tr_b16 a[44:45], %[tq1] offset:10240\n"
        "v_max3_u32 v240, v204, v205, v240\n"
        "v_max3_u32 v240, v206, v207, v240\n"
        "v_mfma_f32_32x32x16_bf16 v[64:79], v[184:187], a[216:219], v[64:79]\n"
        "s_waitcnt lgkmcnt(8)\n"
        "ds_read_b64_tr_b16 a[46:47], %[tq1] offset:11264\n"
        "buffer_load_dwordx4 v[184:187], %[va], %[srda], %[so1] offen offset:2048 sc1\n"
        "buffer_load_dwordx4 v[188:191], %[vb], %[srda], %[so1] offen offset:2048 sc1\n"
        "v_cmp_eq_u32 vcc, -1, v240\n"
        "s_cbranch_vccnz .Ls2_%=\n"
        ".Lb2_%=:\n"
        "v_mfma_f32_32x32x16_bf16 v[64:79], v[192:195], a[224:227], v[64:79]\n"
        "ds_read_b64_tr_b16 a[48:49], %[tq0] offset:12288\n"
        "v_mfma_f32_32x32x16_bf16 v[80:95], v[196:199], a[224:227], v[80:95]\n"
        "ds_read_b64_tr_b16 a[50:51], %[tq0] offset:13312\n"
        "s_waitcnt vmcnt(10)\n"
        "v_mfma_f32_32x32x16_bf16 v[96:111], v[192:195], a[228:231], v[96:111]\n"
        "ds_read_b64_tr_b16 a[52:53], %[tq1] offset:12288\n"
        "v_max_u32 v240, v208, v209\n"
        "v_max3_u32 v240, v210, v211, v240\n"
        "v_mfma_f32_32x32x16_bf16 v[112:127], v[196:199], a[228:231], v[112:127]\n"
        "ds_read_b64_tr_b16 a[54:55], %[tq1] offset:13312\n"
        "v_max3_u32 v240, v212, v213, v240\n"
        "v_max3_u32 v240, v214, v215, v240\n"
        "s_add_u32 %[so2], %[so1], 4096\n"
        "s_cmp_eq_u32 %[so2], %[soae]\n"
        "s_cselect_b32 %[so2], %[soas], %[so2]\n"
        "buffer_load_dwordx4 v[192:195], %[va], %[srda], %[so2] offen sc1\n"
        "buffer_load_dwordx4 v[196:199], %[vb], %[srda], %[so2] offen sc1\n"
        "v_mfma_f32_32x32x16_bf16 v[80:95], v[204:207], a[232:235], v[80:95]\n"
        "ds_read_b64_tr_b16 a[56:57], %[tq0] offset:14336\n"
        "s_waitcnt vmcnt(10)\n"
        "v_mfma_f32_32x32x16_bf16 v[96:111], v[200:203], a[236:239], v[96:111]\n"
        "ds_read_b64_tr_b16 a[58:59], %[tq0] offset:15360\n"
        "v_max3_u32 v240, v216, v217, v240\n"
        "v_max3_u32 v240, v218, v219, v240\n"
        "v_mfma_f32_32x32x16_bf16 v[112:127], v[204:207], a[236:239], v[112:127]\n"
        "s_waitcnt lgkmcnt(8)\n"
        "ds_read_b64_tr_b16 a[60:61], %[tq1] offset:14336\n"
        "v_max3_u32 v240, v220, v221, v240\n"
        "v_max3_u32 v240, v222, v223, v240\n"
        "v_mfma_f32_32x32x16_bf16 v[64:79], v[200:203], a[232:235], v[64:79]\n"
        "ds_read_b64_tr_b16 a[62:63], %[tq1] offset:15360\n"
        "buffer_load_dwordx4 v[200:203], %[va], %[srda], %[so2] offen offset:2048 sc1\n"
        "buffer_load_dwordx4 v[204:207], %[vb], %[srda], %[so2] offen offset:2048 sc1\n"
        "v_cmp_eq_u32 vcc, -1, v240\n"
        "s_cbranch_vccnz .Ls3_%=\n"
        ".Lb3_%=:\n"
        "v_mfma_f32_32x32x16_bf16 v[64:79], v[208:211], a[240:243], v[64:79]\n"
        "ds_read_b64_tr_b16 a[64:65], %[tq0] offset:16384\n"
        "v_mfma_f32_32x32x16_bf16 v[80:95], v[212:215], a[240:243], v[80:95]\n"
        "ds_read_b64_tr_b16 a[66:67], %[tq0] offset:17408\n"
        "s_waitcnt vmcnt(10)\n"
        "v_mfma_f32_32x32x16_bf16 v[96:111], v[208:211], a[244:247], v[96:111]\n"
        "ds_read_b64_tr_b16 a[68:69], %[tq1] offset:16384\n"
        "v_max_u32 v240, v160, v161\n"
        "v_max3_u32 v240, v162, v163, v240\n"
        "v_mfma_f32_32x32x16_bf16 v[112:127], v[212:215], a[244:247], v[112:127]\n"
        "ds_read_b64_tr_b16 a[70:71], %[tq1] offset:17408\n"
        "v_max3_u32 v240, v164, v165, v240\n"
        "v_max3_u32 v240, v166, v167, v240\n"
        "s_add_u32 %[so3], %[so2], 4096\n"
        "s_cmp_eq_u32 %[so3], %[soae]\n"
        "s_cselect_b32 %[so3], %[soas], %[so3]\n"
        "buffer_load_dwordx4 v[208:211], %[va], %[srda], %[so3] offen sc1\n"
        "buffer_load_dwordx4 v[212:215], %[vb], %[srda], %[so3] offen sc1\n"
        "v_mfma_f32_32x32x16_bf16 v[80:95], v[220:223], a[248:251], v[80:95]\n"
        "ds_read_b64_tr_b16 a[72:73], %[tq0] offset:18432\n"
        "s_waitcnt vmcnt(10)\n"
        "v_mfma_f32_32x32x16_bf16 v[96:111], v[216:219], a[252:255], v[96:111]\n"
        "s_waitcnt lgkmcnt(8)\n"
        "ds_read_b64_tr_b16 a[74:75], %[tq0] offset:19456\n"
        "v_max3_u32 v240, v168, v169, v240\n"
        "v_max3_u32 v240, v170, v171, v240\n"
        "v_mfma_f32_32x32x16_bf16 v[112:127], v[220:223], a[252:255], v[112:127]\n"
        "ds_read_b64_tr_b16 a[76:77], %[tq1] offset:18432\n"
        "v_max3_u32 v240, v172, v173, v240\n"
        "v_max3_u32 v240, v174, v175, v240\n"
        "v_mfma_f32_32x32x16_bf16 v[64:79], v[216:219], a[248:251], v[64:79]\n"
        "ds_read_b64_tr_b16 a[78:79], %[tq1] offset:19456\n"
        "buffer_load_dwordx4 v[216:219], %[va], %[srda], %[so3] offen offset:2048 sc1\n"
        "buffer_load_dwordx4 v[220:223], %[vb], %[srda], %[so3] offen offset:2048 sc1\n"
        "v_cmp_eq_u32 vcc, -1, v240\n"
        "s_cbranch_vccnz .Ls4_%=\n"
        ".Lb4_%=:\n"
        "v_mfma_f32_32x32x16_bf16 v[64:79], v[160:163], a[0:3], v[64:79]\n"
        "ds_read_b64_tr_b16 a[80:81], %[tq0] offset:0\n"
        "v_mfma_f32_32x32x16_bf16 v[80:95], v[164:167], a[0:3], v[80:95]\n"
        "ds_read_b64_tr_b16 a[82:83], %[tq0] offset:1024\n"
        "s_waitcnt vmcnt(10)\n"
        "v_mfma_f32_32x32x16_bf16 v[96:111], v[160:163], a[4:7], v[96:111]\n"
        "ds_read_b64_tr_b16 a[84:85], %[tq1] offset:0\n"
        "v_max_u32 v240, v176, v177\n"
        "v_max3_u32 v240, v178, v179, v240\n"
        "v_mfma_f32_32x32x16_bf16 v[112:127], v[164:167], a[4:7], v[112:127]\n"
        "ds_read_b64_tr_b16 a[86:87], %[tq1] offset:1024\n"
        "v_max3_u32 v240, v180, v181, v240\n"
        "v_max3_u32 v240, v182, v183, v240\n"
        "s_add_u32 %[so0], %[so3], 4096\n"
        "s_cmp_eq_u32 %[so0], %[soae]\n"
        "s_cselect_b32 %[so0], %[soas], %[so0]\n"
        "buffer_load_dwordx4 v[160:163], %[va], %[srda], %[so0] offen sc1\n"
        "buffer_load_dwordx4 v[164:167], %[vb], %[srda], %[so0] offen sc1\n"
        "v_mfma_f32_32x32x16_bf16 v[80:95], v[172:175], a[8:11], v[80:95]\n"
        "s_waitcnt lgkmcnt(8)\n"
        "ds_read_b64_tr_b16 a[88:89], %[tq0] offset:2048\n"
        "s_waitcnt vmcnt(10)\n"
        "v_mfma_f32_32x32x16_bf16 v[96:111], v[168:171], a[12:15], v[96:111]\n"
        "ds_read_b64_tr_b16 a[90:91], %[tq0] offset:3072\n"
        "v_max3_u32 v240, v184, v185, v240\n"
        "v_max3_u32 v240, v186, v187, v240\n"
        "v_mfma_f32_32x32x16_bf16 v[112:127], v[172:175], a[12:15], v[112:127]\n"
        "ds_read_b64_tr_b16 a[92:93], %[tq1] offset:2048\n"
        "v_max3_u32 v240, v188, v189, v240\n"
        "v_max3_u32 v240, v190, v191, v240\n"
        "v_mfma_f32_32x32x16_bf16 v[64:79], v[168:171], a[8:11], v[64:79]\n"
        "ds_read_b64_tr_b16 a[94:95], %[tq1] offset:3072\n"
        "buffer_load_dwordx4 v[168:171], %[va], %[srda], %[so0] offen offset:2048 sc1\n"
        "buffer_load_dwordx4 v[172:175], %[vb], %[srda], %[so0] offen offset:2048 sc1\n"
        "v_cmp_eq_u32 vcc, -1, v240\n"
        "s_cbranch_vccnz .Ls5_%=\n"
        ".Lb5_%=:\n"
        "v_mfma_f32_32x32x16_bf16 v[64:79], v[176:179], a[16:19], v[64:79]\n"
        "ds_read_b64_tr_b16 a[96:97], %[tqy0] offset:0\n"
        "v_mfma_f32_32x32x16_bf16 v[80:95], v[180:183], a[16:19], v[80:95]\n"
        "ds_read_b64_tr_b16 a[98:99], %[tqy0] offset:1024\n"
        "s_waitcnt vmcnt(10)\n"
        "v_mfma_f32_32x32x16_bf16 v[96:111], v[176:179], a[20:23], v[96:111]\n"
        "ds_read_b64_tr_b16 a[100:101], %[tqy1] offset:0\n"
        "v_max_u32 v240, v192, v193\n"
        "v_max3_u32 v240, v194, v195, v240\n"
        "v_mfma_f32_32x32x16_bf16 v[112:127], v[180:183], a[20:23], v[112:127]\n"
        "s_waitcnt lgkmcnt(8)\n"
        "ds_read_b64_tr_b16 a[102:103], %[tqy1] offset:1024\n"
        "v_max3_u32 v240, v196, v197, v240\n"
        "v_max3_u32 v240, v198, v199, v240\n"
        "s_add_u32 %[so1], %[so0], 4096\n"
        "s_cmp_eq_u32 %[so1], %[soae]\n"
        "s_cselect_b32 %[so1], %[soas], %[so1]\n"
        "buffer_load_dwordx4 v[176:179], %[va], %[srda], %[so1] offen sc1\n"
        "buffer_load_dwordx4 v[180:183], %[vb], %[srda], %[so1] offen sc1\n"
        "v_mfma_f32_32x32x16_bf16 v[80:95], v[188:191], a[24:27], v[80:95]\n"
        "ds_read_b64_tr_b16 a[104:105], %[tqy0] offset:2048\n"
        "s_waitcnt vmcnt(10)\n"
        "v_mfma_f32_32x32x16_bf16 v[96:111], v[184:187], a[28:31], v[96:111]\n"
        "ds_read_b64_tr_b16 a[106:107], %[tqy0] offset:3072\n"
        "v_max3_u32 v240, v200, v201, v240\n"
        "v_max3_u32 v240, v202, v203, v240\n"
        "v_mfma_f32_32x32x16_bf16 v[112:127], v[188:191], a[28:31], v[112:127]\n"
        "ds_read_b64_tr_b16 a[108:109], %[tqy1] offset:2048\n"
        "v_max3_u32 v240, v204, v205, v240\n"
        "v_max3_u32 v240, v206, v207, v240\n"
        "v_mfma_f32_32x32x16_bf16 v[64:79], v[184:187], a[24:27], v[64:79]\n"
        "ds_read_b64_tr_b16 a[110:111], %[tqy1] offset:3072\n"
        "buffer_load_dwordx4 v[184:187], %[va], %[srda], %[so1] offen offset:2048 sc1\n"
        "buffer_load_dwordx4 v[188:191], %[vb], %[srda], %[so1] offen offset:2048 sc1\n"
        "v_cmp_eq_u32 vcc, -1, v240\n"
        "s_cbranch_vccnz .Ls6_%=\n"
        ".Lb6_%=:\n"
        "v_mfma_f32_32x32x16_bf16 v[64:79], v[192:195], a[32:35], v[64:79]\n"
        "v_mfma_f32_32x32x16_bf16 v[80:95], v[196:199], a[32:35], v[80:95]\n"
        "s_waitcnt vmcnt(10)\n"
        "v_mfma_f32_32x32x16_bf16 v[96:111], v[192:195], a[36:39], v[96:111]\n"
        "v_max_u32 v240, v208, v209\n"
        "v_max3_u32 v240, v210, v211, v240\n"
        "v_mfma_f32_32x32x16_bf16 v[112:127], v[196:199], a[36:39], v[112:127]\n"
        "v_max3_u32 v240, v212, v213, v240\n"
        "v_max3_u32 v240, v214, v215, v240\n"
        "s_add_u32 %[so2], %[so1], 4096\n"
        "s_cmp_eq_u32 %[so2], %[soae]\n"
        "s_cselect_b32 %[so2], %[soas], %[so2]\n"
        "buffer_load_dwordx4 v[192:195], %[va], %[srda], %[so2] offen sc1\n"
        "buffer_load_dwordx4 v[196:199], %[vb], %[srda], %[so2] offen sc1\n"
        "v_mfma_f32_32x32x16_bf16 v[80:95], v[204:207], a[40:43], v[80:95]\n"
        "s_waitcnt vmcnt(10)\n"
        "v_mfma_f32_32x32x16_bf16 v[96:111], v[200:203], a[44:47], v[96:111]\n"
        "v_max3_u32 v240, v216, v217, v240\n"
        "v_max3_u32 v240, v218, v219, v240\n"
        "v_mfma_f32_32x32x16_bf16 v[112:127], v[204:207], a[44:47], v[112:127]\n"
        "v_max3_u32 v240, v220, v221, v240\n"
        "v_max3_u32 v240, v222, v223, v240\n"
        "v_mfma_f32_32x32x16_bf16 v[64:79], v[200:203], a[40:43], v[64:79]\n"
        "buffer_load_dwordx4 v[200:203], %[va], %[srda], %[so2] offen offset:2048 sc1\n"
        "buffer_load_dwordx4 v[204:207], %[vb], %[srda], %[so2] offen offset:2048 sc1\n"
        "v_cmp_eq_u32 vcc, -1, v240\n"
        "s_cbranch_vccnz .Ls7_%=\n"
        ".Lb7_%=:\n"
        "v_mfma_f32_32x32x16_bf16 v[64:79], v[208:211], a[48:51], v[64:79]\n"
        "v_mfma_f32_32x32x16_bf16 v[80:95], v[212:215], a[48:51], v[80:95]\n"
        "s_waitcnt vmcnt(10)\n"
        "v_mfma_f32_32x32x16_bf16 v[96:111], v[208:211], a[52:55], v[96:111]\n"
        "v_max_u32 v240, v160, v161\n"
        "v_max3_u32 v240, v162, v163, v240\n"
        "v_mfma_f32_32x32x16_bf16 v[112:127], v[212:215], a[52:55], v[112:127]\n"
        "v_max3_u32 v240, v164, v165, v240\n"
        "v_max3_u32 v240, v166, v167, v240\n"
        "v_mfma_f32_32x32x16_bf16 v[80:95], v[220:223], a[56:59], v[80:95]\n"
        "s_waitcnt vmcnt(8)\n"
        "v_mfma_f32_32x32x16_bf16 v[96:111], v[216:219], a[60:63], v[96:111]\n"
        "v_max3_u32 v240, v168, v169, v240\n"
        "v_max3_u32 v240, v170, v171, v240\n"
        "v_mfma_f32_32x32x16_bf16 v[112:127], v[220:223], a[60:63], v[112:127]\n"
        "v_max3_u32 v240, v172, v173, v240\n"
        "v_max3_u32 v240, v174, v175, v240\n"
        "v_mfma_f32_32x32x16_bf16 v[64:79], v[216:219], a[56:59], v[64:79]\n"
        "v_cmp_eq_u32 vcc, -1, v240\n"
        "s_cbranch_vccnz .Ls8_%=\n"
        ".Lb8_%=:\n"
        "v_mfma_f32_32x32x16_bf16 v[64:79], v[160:163], a[64:67], v[64:79]\n"
        "v_mfma_f32_32x32x16_bf16 v[80:95], v[164:167], a[64:67], v[80:95]\n"
        "s_waitcnt vmcnt(6)\n"
        "v_mfma_f32_32x32x16_bf16 v[96:111], v[160:163], a[68:71], v[96:111]\n"
        "v_max_u32 v240, v176, v177\n"
        "v_max3_u32 v240, v178, v179, v240\n"
        "v_mfma_f32_32x32x16_bf16 v[112:127], v[164:167], a[68:71], v[112:127]\n"
        "v_max3_u32 v240, v180, v181, v240\n"
        "v_max3_u32 v240, v182, v183, v240\n"
        "v_mfma_f32_32x32x16_bf16 v[80:95], v[172:175], a[72:75], v[80:95]\n"
        "s_waitcnt vmcnt(4)\n"
        "v_mfma_f32_32x32x16_bf16 v[96:111], v[168:171], a[76:79], v[96:111]\n"
        "v_max3_u32 v240, v184, v185, v240\n"
        "v_max3_u32 v240, v186, v187, v240\n"
        "v_mfma_f32_32x32x16_bf16 v[112:127], v[172:175], a[76:79], v[112:127]\n"
        "v_max3_u32 v240, v188, v189, v240\n"
        "v_max3_u32 v240, v190, v191, v240\n"
        "v_mfma_f32_32x32x16_bf16 v[64:79], v[168:171], a[72:75], v[64:79]\n"
        "v_cmp_eq_u32 vcc, -1, v240\n"
        "s_cbranch_vccnz .Ls9_%=\n"
        ".Lb9_%=:\n"
        "s_waitcnt lgkmcnt(0)\n"
        "v_mfma_f32_32x32x16_bf16 v[80:95], v[180:183], a[80:83], v[80:95]\n"
        "s_waitcnt vmcnt(0)\n"
        "v_max_u32 v240, v192, v193\n"
        "v_max3_u32 v240, v194, v195, v240\n"
        "v_max3_u32 v240, v196, v197, v240\n"
        "v_max3_u32 v240, v198, v199, v240\n"
        "v_mfma_f32_32x32x16_bf16 v[80:95], v[188:191], a[88:91], v[80:95]\n"
        "v_max3_u32 v240, v200, v201, v240\n"
        "v_max3_u32 v240, v202, v203, v240\n"
        "v_max3_u32 v240, v204, v205, v240\n"
        "v_max3_u32 v240, v206, v207, v240\n"
        "v_mfma_f32_32x32x16_bf16 v[96:111], v[176:179], a[84:87], v[96:111]\n"
        "v_cmp_eq_u32 vcc, -1, v240\n"
        "s_cbranch_vccnz .Ls10_%=\n"
        ".Lb10_%=:\n"
        "v_mfma_f32_32x32x16_bf16 v[80:95], v[196:199], a[96:99], v[80:95]\n"
        "v_mfma_f32_32x32x16_bf16 v[80:95], v[204:207], a[104:107], v[80:95]\n"
        "v_mfma_f32_32x32x16_bf16 v[96:111], v[184:187], a[92:95], v[96:111]\n"
        "v_mfma_f32_32x32x16_bf16 v[96:111], v[192:195], a[100:103], v[96:111]\n"
        "v_mfma_f32_32x32x16_bf16 v[96:111], v[200:203], a[108:111], v[96:111]\n"
        "s_nop 7\n"
        "v_mfma_f32_32x32x16_bf16 v[112:127], v[180:183], a[84:87], v[112:127]\n"
        "ds_write_b128 %[xw1], v[80:83] offset:0\n"
        "v_mfma_f32_32x32x16_bf16 v[112:127], v[188:191], a[92:95], v[112:127]\n"
        "ds_write_b128 %[xw1], v[84:87] offset:1024\n"
        "v_mfma_f32_32x32x16_bf16 v[112:127], v[196:199], a[100:103], v[112:127]\n"
        "ds_write_b128 %[xw1], v[88:91] offset:2048\n"
        "v_mfma_f32_32x32x16_bf16 v[112:127], v[204:207], a[108:111], v[112:127]\n"
        "ds_write_b128 %[xw1], v[92:95] offset:3072\n"
        "v_mfma_f32_32x32x16_bf16 v[64:79], v[176:179], a[80:83], v[64:79]\n"
        "ds_write_b128 %[xw2], v[96:99] offset:4096\n"
        "v_mfma_f32_32x32x16_bf16 v[64:79], v[184:187], a[88:91], v[64:79]\n"
        "ds_write_b128 %[xw2], v[100:103] offset:5120\n"
        "v_mfma_f32_32x32x16_bf16 v[64:79], v[192:195], a[96:99], v[64:79]\n"
        "ds_write_b128 %[xw2], v[104:107] offset:6144\n"
        "v_mfma_f32_32x32x16_bf16 v[64:79], v[200:203], a[104:107], v[64:79]\n"
        "ds_write_b128 %[xw2], v[108:111] offset:7168\n"
        "s_nop 3\n"
        "ds_write_b128 %[xw3], v[112:115] offset:8192\n"
        "ds_write_b128 %[xw3], v[116:119] offset:9216\n"
        "ds_write_b128 %[xw3], v[120:123] offset:10240\n"
        "ds_write_b128 %[xw3], v[124:127] offset:11264\n"
        STAMP_ASM(11)
        "s_waitcnt lgkmcnt(0)\n"
        "s_barrier\n"
        "ds_read_b128 v[80:83], %[xr] offset:0\n"
        "ds_read_b128 v[84:87], %[xr] offset:1024\n"
        "ds_read_b128 v[88:91], %[xr] offset:2048\n"
        "ds_read_b128 v[92:95], %[xr] offset:3072\n"
        "ds_read_b128 v[96:99], %[xr] offset:4096\n"
        "ds_read_b128 v[100:103], %[xr] offset:5120\n"
        "ds_read_b128 v[104:107], %[xr] offset:6144\n"
        "ds_read_b128 v[108:111], %[xr] offset:7168\n"
        "ds_read_b128 v[112:115], %[xr] offset:8192\n"
        "ds_read_b128 v[116:119], %[xr] offset:9216\n"
        "ds_read_b128 v[120:123], %[xr] offset:10240\n"
        "ds_read_b128 v[124:127], %[xr] offset:11264\n"
        "s_waitcnt lgkmcnt(8)\n"
        "v_pk_add_f32 v[64:65], v[64:65], v[80:81]\n"
        "v_pk_add_f32 v[66:67], v[66:67], v[82:83]\n"
        "v_pk_add_f32 v[68:69], v[68:69], v[84:85]\n"
        "v_pk_add_f32 v[70:71], v[70:71], v[86:87]\n"
        "v_pk_add_f32 v[72:73], v[72:73], v[88:89]\n"
        "v_pk_add_f32 v[74:75], v[74:75], v[90:91]\n"
        "v_pk_add_f32 v[76:77], v[76:77], v[92:93]\n"
        "v_pk_add_f32 v[78:79], v[78:79], v[94:95]\n"
        "s_waitcnt lgkmcnt(4)\n"
        "v_pk_add_f32 v[64:65], v[64:65], v[96:97]\n"
        "v_pk_add_f32 v[66:67], v[66:67], v[98:99]\n"
        "v_pk_add_f32 v[68:69], v[68:69], v[100:101]\n"
        "v_pk_add_f32 v[70:71], v[70:71], v[102:103]\n"
        "v_pk_add_f32 v[72:73], v[72:73], v[104:105]\n"
        "v_pk_add_f32 v[74:75], v[74:75], v[106:107]\n"
        "v_pk_add_f32 v[76:77], v[76:77], v[108:109]\n"
        "v_pk_add_f32 v[78:79], v[78:79], v[110:111]\n"
        "s_waitcnt lgkmcnt(0)\n"
        "v_pk_add_f32 v[64:65], v[64:65], v[112:113]\n"
        "v_pk_add_f32 v[66:67], v[66:67], v[114:115]\n"
        "v_pk_add_f32 v[68:69], v[68:69], v[116:117]\n"
        "v_pk_add_f32 v[70:71], v[70:71], v[118:119]\n"
        "v_pk_add_f32 v[72:73], v[72:73], v[120:121]\n"
        "v_pk_add_f32 v[74:75], v[74:75], v[122:123]\n"
        "v_pk_add_f32 v[76:77], v[76:77], v[124:125]\n"
        "v_pk_add_f32 v[78:79], v[78:79], v[126:127]\n"
        "s_branch .Lz%=\n"
        ".Ls0_%=:\n"
        "s_cmp_eq_u32 0, 0\n"
        "s_getpc_b64 %[ret]\n"
        "s_cbranch_scc1 .Lw%=\n"
        "s_branch .Lb0_%=\n"
        ".Ls1_%=:\n"
        "s_cmp_eq_u32 0, 0\n"
        "s_getpc_b64 %[ret]\n"
        "s_cbranch_scc1 .Lw%=\n"
        "s_branch .Lb1_%=\n"
        ".Ls2_%=:\n"
        "s_cmp_eq_u32 0, 0\n"
        "s_getpc_b64 %[ret]\n"
        "s_cbranch_scc1 .Lw%=\n"
        "s_branch .Lb2_%=\n"
        ".Ls3_%=:\n"
        "s_cmp_eq_u32 0, 0\n"
        "s_getpc_b64 %[ret]\n"
        "s_cbranch_scc1 .Lw%=\n"
        "s_branch .Lb3_%=\n"
        ".Ls4_%=:\n"
        "s_cmp_eq_u32 0, 0\n"
        "s_getpc_b64 %[ret]\n"
        "s_cbranch_scc1 .Lw%=\n"
        "s_branch .Lb4_%=\n"
        ".Ls5_%=:\n"
        "s_cmp_eq_u32 0, 0\n"
        "s_getpc_b64 %[ret]\n"
        "s_cbranch_scc1 .Lw%=\n"
        "s_branch .Lb5_%=\n"
        ".Ls6_%=:\n"
        "s_cmp_eq_u32 0, 0\n"
        "s_getpc_b64 %[ret]\n"
        "s_cbranch_scc1 .Lw%=\n"
        "s_branch .Lb6_%=\n"
        ".Ls7_%=:\n"
        "s_cmp_eq_u32 0, 0\n"
        "s_getpc_b64 %[ret]\n"
        "s_cbranch_scc1 .Lw%=\n"
        "s_branch .Lb7_%=\n"
        ".Ls8_%=:\n"
        "s_cmp_eq_u32 0, 0\n"
        "s_getpc_b64 %[ret]\n"
        "s_cbranch_scc1 .Lw%=\n"
        "s_branch .Lb8_%=\n"
        ".Ls9_%=:\n"
        "s_cmp_eq_u32 0, 0\n"
        "s_getpc_b64 %[ret]\n"
        "s_cbranch_scc1 .Lw%=\n"
        "s_branch .Lb9_%=\n"
        ".Ls10_%=:\n"
        "s_cmp_eq_u32 0, 0\n"
        "s_getpc_b64 %[ret]\n"
        "s_cbranch_scc1 .Lw%=\n"
        "s_branch .Lb10_%=\n"
        ".Lw%=:\n"
        "s_mov_b32 %[tmp], 0x40000\n"
        ".Lv%=:\n"
        "s_waitcnt vmcnt(0)\n"
        "s_mov_b32 %[bad], 0\n"
        "v_max_u32 v240, v160, v161\n"
        "v_max3_u32 v240, v162, v163, v240\n"
        "v_max3_u32 v240, v164, v165, v240\n"
        "v_max3_u32 v240, v166, v167, v240\n"
        "v_max3_u32 v240, v168, v169, v240\n"
        "v_max3_u32 v240, v170, v171, v240\n"
        "v_max3_u32 v240, v172, v173, v240\n"
        "v_max3_u32 v240, v174, v175, v240\n"
        "v_cmp_eq_u32 vcc, -1, v240\n"
        "s_cbranch_vccz .Lu0_%=\n"
        "buffer_load_dwordx4 v[160:163], %[va], %[srda], %[so0] offen sc1\n"
        "buffer_load_dwordx4 v[164:167], %[vb], %[srda], %[so0] offen sc1\n"
        "buffer_load_dwordx4 v[168:171], %[va], %[srda], %[so0] offen offset:2048 sc1\n"
        "buffer_load_dwordx4 v[172:175], %[vb], %[srda], %[so0] offen offset:2048 sc1\n"
        "s_mov_b32 %[bad], 1\n"
        ".Lu0_%=:\n"
        "v_max_u32 v240, v176, v177\n"
        "v_max3_u32 v240, v178, v179, v240\n"
        "v_max3_u32 v240, v180, v181, v240\n"
        "v_max3_u32 v240, v182, v183, v240\n"
        "v_max3_u32 v240, v184, v185, v240\n"
        "v_max3_u32 v240, v186, v187, v240\n"
        "v_max3_u32 v240, v188, v189, v240\n"
        "v_max3_u32 v240, v190, v191, v240\n"
        "v_cmp_eq_u32 vcc, -1, v240\n"
        "s_cbranch_vccz .Lu1_%=\n"
        "buffer_load_dwordx4 v[176:179], %[va], %[srda], %[so1] offen sc1\n"
        "buffer_load_dwordx4 v[180:183], %[vb], %[srda], %[so1] offen sc1\n"
        "buffer_load_dwordx4 v[184:187], %[va], %[srda], %[so1] offen offset:2048 sc1\n"
        "buffer_load_dwordx4 v[188:191], %[vb], %[srda], %[so1] offen offset:2048 sc1\n"
        "s_mov_b32 %[bad], 1\n"
        ".Lu1_%=:\n"
        "v_max_u32 v240, v192, v193\n"
        "v_max3_u32 v240, v194, v195, v240\n"
        "v_max3_u32 v240, v196, v197, v240\n"
        "v_max3_u32 v240, v198, v199, v240\n"
        "v_max3_u32 v240, v200, v201, v240\n"
        "v_max3_u32 v240, v202, v203, v240\n"
        "v_max3_u32 v240, v204, v205, v240\n"
        "v_max3_u32 v240, v206, v207, v240\n"
        "v_cmp_eq_u32 vcc, -1, v240\n"
        "s_cbranch_vccz .Lu2_%=\n"
        "buffer_load_dwordx4 v[192:195], %[va], %[srda], %[so2] offen sc1\n"
        "buffer_load_dwordx4 v[196:199], %[vb], %[srda], %[so2] offen sc1\n"
        "buffer_load_dwordx4 v[200:203], %[va], %[srda], %[so2] offen offset:2048 sc1\n"
        "buffer_load_dwordx4 v[204:207], %[vb], %[srda], %[so2] offen offset:2048 sc1\n"
        "s_mov_b32 %[bad], 1\n"
        ".Lu2_%=:\n"
        "v_max_u32 v240, v208, v209\n"
        "v_max3_u32 v240, v210, v211, v240\n"
        "v_max3_u32 v240, v212, v213, v240\n"
        "v_max3_u32 v240, v214, v215, v240\n"
        "v_max3_u32 v240, v216, v217, v240\n"
        "v_max3_u32 v240, v218, v219, v240\n"
        "v_max3_u32 v240, v220, v221, v240\n"
        "v_max3_u32 v240, v222, v223, v240\n"
        "v_cmp_eq_u32 vcc, -1, v240\n"
        "s_cbranch_vccz .Lu3_%=\n"
        "buffer_load_dwordx4 v[208:211], %[va], %[srda], %[so3] offen sc1\n"
        "buffer_load_dwordx4 v[212:215], %[vb], %[srda], %[so3] offen sc1\n"
        "buffer_load_dwordx4 v[216:219], %[va], %[srda], %[so3] offen offset:2048 sc1\n"
        "buffer_load_dwordx4 v[220:223], %[vb], %[srda], %[so3] offen offset:2048 sc1\n"
        "s_mov_b32 %[bad], 1\n"
        ".Lu3_%=:\n"
        "s_cmp_eq_u32 %[bad], 0\n"
        "s_cbranch_scc1 .Lt%=\n"
        "s_sleep 5\n"
        "s_sub_u32 %[tmp], %[tmp], 1\n"
        "s_cbranch_scc0 .Lv%=\n"
        "s_waitcnt vmcnt(0)\n"
        ".Lt%=:\n"
        "s_cmp_eq_u32 0, 1\n"
        "s_setpc_b64 %[ret]\n"
        ".Lz%=:\n"
        : [so0] "+s"(so0), [so1] "=&s"(so1), [so2] "=&s"(so2), [so3] "=&s"(so3), [bad] "=&s"(bad), [ret] "=&s"(ret), [tmp] "=&s"(tmp),
          "=&{v[64:79]}"(acc)
        : [tq0] "v"(tq0), [tq1] "v"(tq1), [tqy0] "v"(tqy0), [tqy1] "v"(tqy1), [va] "v"(va), [vb] "v"(vb), [srda] "s"(srdG), [soae] "s"(sge), [soas] "s"(sgs), [b2v] "v"(b2v),
          [xw1] "v"(xw1), [xw2] "v"(xw2), [xw3] "v"(xw3), [xr] "v"(xr2)
        : "memory", "scc", "vcc", VCLOB_ALL, ACLOB_ALL);
    STAMP(12);
#pragma unroll
    for (int k = 0; k < 4; ++k)
      *(LAS u32x2*)(uintptr_t)(ow + k * 16) = u32x2{pk_bf16(acc[k], acc[4 + k]), pk_bf16(acc[8 + k], acc[12 + k])};
    u32x4 r0, r1;
    asm volatile(
        "s_waitcnt lgkmcnt(0)\n s_barrier\n"
        "ds_read_b128 %[r0], %[orl]\n"
        "ds_read_b128 %[r1], %[orl] offset:1152\n"
        "s_movk_i32 %[tmp], 0x2000\n"
        "s_waitcnt lgkmcnt(1)\n"
        "buffer_store_dwordx4 %[r0], %[og], %[srdo], 0 offen\n"
        "s_waitcnt lgkmcnt(0)\n"
        "buffer_store_dwordx4 %[r1], %[og], %[srdo], %[tmp] offen\n"
        : [r0] "=&v"(r0), [r1] "=&v"(r1), [tmp] "=&s"(tmp) : [orl] "v"(orl), [og] "v"(og), [srdo] "s"(srdo) : "memory");
  }
  asm volatile(".Lcode_z:" ::: "memory");
}

extern "C" void kernel_launch(void* const* d_in, const int* in_sizes, int n_in, void* d_out, int out_size, void* d_ws,
                              size_t ws_size, hipStream_t stream) {
  (void)hipFuncSetAttribute((const void*)mixer_kernel, hipFuncAttributeMaxDynamicSharedMemorySize, LDS_BYTES);
  mixer_kernel<<<256, 256, LDS_BYTES, stream>>>(d_in[0], d_in[1], d_in[2], d_in[3], d_in[4], d_out, d_ws, 0ull);
}
```
